```python
import torch

def _softmax(x, axis=-1):
    return torch.softmax(x, dim=axis)
def _gelu(x):
    return torch.nn.functional.gelu(x, approximate="tanh")
def _silu(x):
    return torch.nn.functional.silu(x)
def _sigmoid(x):
    return torch.sigmoid(x)
def _layernorm(x, axis=-1, eps=1e-5):
    m = x.mean(dim=axis, keepdim=True)
    v = x.var(dim=axis, keepdim=True, unbiased=False)
    return (x - m) / torch.sqrt(v + eps)
def _scatter_add(tgt, idx, src, axis=0):
    return tgt.clone().index_add_(axis, idx, src)
def _segment_sum(src, seg, n):
    out = torch.zeros((n,) + src.shape[1:], dtype=src.dtype)
    return out.index_add_(0, seg, src)
def _pad(x, pw):
    flat = tuple(v for pair in reversed(pw) for v in pair)
    return torch.nn.functional.pad(x, flat)

kernel_name = 'sequential_preLN__linear_attn_block__dense_activation_gelu__prompt'

def setup_inputs():

    _w = (lambda a: (lambda t: t.to(torch.bfloat16) if t.is_floating_point() else t)(torch.as_tensor(a)))
    def _A_setup():
        B, S, D = 1, 2048, 448
        return {
            "x": _w((lambda *s: torch.randn(*s, dtype=torch.bfloat16) * 0.05)(B, S, D)),
            "wqkv": _w((lambda *s: torch.randn(*s, dtype=torch.bfloat16) * 0.1)(D, 3 * D)),
            "wo": _w((lambda *s: torch.randn(*s, dtype=torch.bfloat16) * 0.05)(D, D)),
        }
    _A_in = _A_setup()

    def _B_setup():
        B, S, D, F = 1, 2048, 448, 1792
        return {
            "x": _w((lambda *s: torch.randn(*s, dtype=torch.bfloat16) * 0.05)(B, S, D)),
            "w1": _w((lambda *s: torch.randn(*s, dtype=torch.bfloat16) * 0.8)(D, F) * (D ** -0.5)),
            "b1": _w((lambda *s: torch.randn(*s, dtype=torch.bfloat16) * 12.8)(F) * 0.01),
            "w2": _w((lambda *s: torch.randn(*s, dtype=torch.bfloat16) * 0.4)(F, D) * (F ** -0.5)),
            "b2": _w((lambda *s: torch.randn(*s, dtype=torch.bfloat16) * 6.4)(D) * 0.01),
        }
    _B_in = _B_setup()

    return {
        "A_x": _A_in["x"],
        "A_wqkv": _A_in["wqkv"],
        "A_wo": _A_in["wo"],
        "B_x": _B_in["x"],
        "B_w1": _B_in["w1"],
        "B_b1": _B_in["b1"],
        "B_w2": _B_in["w2"],
        "B_b2": _B_in["b2"],
    }

def reference(A_x, A_wqkv, A_wo, B_x, B_w1, B_b1, B_w2, B_b2):
    def _A_ref(x, wqkv, wo):
        B, S, D = x.shape
        H = 16; Dh = D // H
        qkv = (lambda x, *d: x.permute(*d))((x @ wqkv).reshape(B, S, 3, H, Dh), 2, 0, 3, 1, 4)
        q, k, v = qkv[0], qkv[1], qkv[2]
        phi = lambda t: torch.where(t > 0, t + 1, torch.exp(t)) + 1e-6
        pq, pk = phi(q), phi(k)
        kv = torch.einsum("bhsd,bhse->bhde", pk, v)
        z = torch.einsum("bhsd->bhd", pk)
        num = torch.einsum("bhqd,bhde->bhqe", pq, kv)
        den = torch.einsum("bhqd,bhd->bhq", pq, z)[..., None] + 1e-6
        o = num / den
        return (lambda x, *d: x.permute(*d))(o, 0, 2, 1, 3).reshape(B, S, D) @ wo

    def _B_ref(x, w1, b1, w2, b2):
        h = torch.einsum("bsd,df->bsf", x, w1) + b1
        h = _gelu(h)
        return torch.einsum("bsf,fd->bsd", h, w2) + b2

    x = A_x
    h = x + _A_ref(_layernorm(x, axis=-1), A_wqkv, A_wo)
    h2 = h + _B_ref(_layernorm(h, axis=-1), B_w1, B_b1, B_w2, B_b2)
    return h2
```

```cpp
#include <hip/hip_runtime.h>
#include <hip/hip_bf16.h>
#include <stdint.h>

typedef unsigned short u16;
__device__ __forceinline__ float bf2f(u16 b) { return __uint_as_float(((unsigned)b) << 16); }
__device__ __forceinline__ u16 f2bf(float f) { return __builtin_bit_cast(u16, (__bf16)f); }

constexpr int S = 2048, D = 448, H = 16, DH = 28, F = 1792;

__global__ void k_ln(const u16* x, u16* y) {
  int r = blockIdx.x; int t = threadIdx.x;
  __shared__ float red[64];
  float s = 0, ss = 0;
  for (int i = t; i < D; i += 64) { float v = bf2f(x[r * D + i]); s += v; }
  red[t] = s; __syncthreads();
  float tot = 0; for (int i = 0; i < 64; i++) tot += red[i];
  __syncthreads();
  float m = tot / D;
  for (int i = t; i < D; i += 64) { float v = bf2f(x[r * D + i]) - m; ss += v * v; }
  red[t] = ss; __syncthreads();
  tot = 0; for (int i = 0; i < 64; i++) tot += red[i];
  float rs = rsqrtf(tot / D + 1e-5f);
  for (int i = t; i < D; i += 64) y[r * D + i] = f2bf((bf2f(x[r * D + i]) - m) * rs);
}
__global__ void k_gemm(const u16* A, const u16* B, u16* C, int M, int N, int K, int mode, const u16* bias, const u16* res) {
  int n = blockIdx.x * 64 + threadIdx.x; int m = blockIdx.y;
  if (n >= N) return;
  float acc = 0;
  for (int k = 0; k < K; k++) acc += bf2f(A[m * K + k]) * bf2f(B[k * N + n]);
  if (mode == 1) { float v = acc + bf2f(bias[n]); float u = 0.7978845608f * (v + 0.044715f * v * v * v); acc = 0.5f * v * (1.f + tanhf(u)); }
  if (mode == 2) acc = acc + bf2f(res[m * N + n]);
  if (mode == 3) acc = acc + bf2f(bias[n]) + bf2f(res[m * N + n]);
  C[m * N + n] = f2bf(acc);
}
__device__ float phi(float t) { return (t > 0 ? t + 1.f : __expf(t)) + 1e-6f; }
__global__ void k_kv(const u16* qkv, float* kv) {
  int h = blockIdx.x / DH, d = blockIdx.x % DH, e = threadIdx.x;
  if (e > DH) return;
  float acc = 0;
  for (int s = 0; s < S; s++) {
    float pk = bf2f(f2bf(phi(bf2f(qkv[s * 3 * D + D + h * DH + d]))));
    float v = e < DH ? bf2f(qkv[s * 3 * D + 2 * D + h * DH + e]) : 1.f;
    acc += pk * v;
  }
  kv[(h * DH + d) * 32 + e] = acc;
}
__global__ void k_o(const u16* qkv, const float* kv, u16* o) {
  int s = blockIdx.x; int c = threadIdx.x; if (c >= D) return;
  int h = c / DH, e = c % DH;
  float num = 0, den = 0;
  for (int d = 0; d < DH; d++) {
    float pq = bf2f(f2bf(phi(bf2f(qkv[s * 3 * D + h * DH + d]))));
    num += pq * kv[(h * DH + d) * 32 + e];
    den += pq * kv[(h * DH + d) * 32 + DH];
  }
  o[s * D + c] = f2bf(num / (den + 1e-6f));
}

extern "C" void kernel_launch(void* const* d_in, const int* in_sizes, int n_in,
                              void* d_out, int out_size, void* d_ws, size_t ws_size,
                              hipStream_t stream) {
  const u16* x = (const u16*)d_in[0];
  const u16* wqkv = (const u16*)d_in[1];
  const u16* wo = (const u16*)d_in[2];
  const u16* w1 = (const u16*)d_in[4];
  const u16* b1 = (const u16*)d_in[5];
  const u16* w2 = (const u16*)d_in[6];
  const u16* b2 = (const u16*)d_in[7];
  u16* out = (u16*)d_out;
  char* ws = (char*)d_ws;
  u16* ln = (u16*)ws; ws += S * D * 2;
  u16* qkv = (u16*)ws; ws += S * 3 * D * 2;
  float* kv = (float*)ws; ws += H * DH * 32 * 4;
  u16* o = (u16*)ws; ws += S * D * 2;
  u16* h = (u16*)ws; ws += S * D * 2;
  u16* h1 = (u16*)ws; ws += S * F * 2;
  k_ln<<<S, 64, 0, stream>>>(x, ln);
  k_gemm<<<dim3(3 * D / 64, S), 64, 0, stream>>>(ln, wqkv, qkv, S, 3 * D, D, 0, nullptr, nullptr);
  k_kv<<<H * DH, 64, 0, stream>>>(qkv, kv);
  k_o<<<S, 448, 0, stream>>>(qkv, kv, o);
  k_gemm<<<dim3(D / 64, S), 64, 0, stream>>>(o, wo, h, S, D, D, 2, nullptr, x);
  k_ln<<<S, 64, 0, stream>>>(h, ln);
  k_gemm<<<dim3(F / 64, S), 64, 0, stream>>>(ln, w1, h1, S, F, D, 1, b1, nullptr);
  k_gemm<<<dim3(D / 64, S), 64, 0, stream>>>(h1, w2, out, S, D, F, 3, b2, h);
}
```

```cpp
#include <hip/hip_runtime.h>
#include <hip/hip_bf16.h>
#include <stdint.h>

typedef unsigned short u16;
typedef unsigned v4u __attribute__((ext_vector_type(4)));
typedef unsigned v2u __attribute__((ext_vector_type(2)));
typedef float f32x2 __attribute__((ext_vector_type(2)));
typedef float f32x4 __attribute__((ext_vector_type(4)));
typedef float f32x16 __attribute__((ext_vector_type(16)));
typedef __bf16 bf16x8 __attribute__((ext_vector_type(8)));
typedef __bf16 bf16x4 __attribute__((ext_vector_type(4)));
typedef __bf16 bf16x2 __attribute__((ext_vector_type(2)));
typedef short s16x4 __attribute__((ext_vector_type(4)));
#define LAS __attribute__((address_space(3)))
#define DI __device__ __forceinline__
typedef __amdgpu_buffer_rsrc_t rsrc_t;

constexpr int S = 2048, D = 448, F = 1792;
constexpr int WS_CTRL = 0;
constexpr int WS_KVP = 65536;
constexpr int WS_PQ = WS_KVP + 16 * 16 * 4096;
constexpr int WS_OF = WS_PQ + 64 * 32 * 1024;
constexpr int WS_HF = WS_OF + 64 * 32 * 1024;
constexpr int WS_HST = WS_HF + 64 * 28 * 1024;
constexpr int WS_H1F = WS_HST + 64 * 14 * 256;
constexpr int WS_WOT = WS_H1F + 64 * 112 * 1024;
constexpr int WS_W1T = WS_WOT + 14 * 32 * 1024;
constexpr int WS_W2T = WS_W1T + 56 * 28 * 1024;
constexpr int WS_C1P = WS_W2T + 14 * 112 * 1024;
constexpr int WS_C1B = WS_C1P + 14 * 1792 * 4;
constexpr int WS_END = WS_C1B + 56 * 256;
#define STAMP(i)
#define STAMP_DECL
#define STAMP_OUT(lo, hi)

DI f32x16 mfma32(v4u a, v4u b, f32x16 c) {
  return __builtin_amdgcn_mfma_f32_32x32x16_bf16(__builtin_bit_cast(bf16x8, a), __builtin_bit_cast(bf16x8, b), c, 0, 0, 0);
}
DI f32x4 mfma4(unsigned a0, unsigned a1, unsigned b0, unsigned b1, f32x4 c) {
  v2u a = {a0, a1}, b = {b0, b1};
  return __builtin_amdgcn_mfma_f32_4x4x4bf16_1k(__builtin_bit_cast(s16x4, a), __builtin_bit_cast(s16x4, b), c, 0, 0, 0);
}
DI unsigned pk2(float lo, float hi) { bf16x2 v = {(__bf16)lo, (__bf16)hi}; return __builtin_bit_cast(unsigned, v); }
DI float asf(unsigned u) { return __builtin_bit_cast(float, u); }
DI float bflo(unsigned u) { return __builtin_bit_cast(float, u << 16); }
DI float bfhi(unsigned u) { return __builtin_bit_cast(float, u & 0xffff0000u); }
DI float halfsum(float a) {
  unsigned u = __builtin_bit_cast(unsigned, a), u2 = u;
  asm("" : "+v"(u2));
  auto r = __builtin_amdgcn_permlane32_swap(u, u2, false, false);
  const unsigned r0 = r[0], r1 = r[1];
  return __builtin_bit_cast(float, r0) + __builtin_bit_cast(float, r1);
}
DI rsrc_t mkrs(const void* p, int bytes) { return __builtin_amdgcn_make_buffer_rsrc((void*)p, 0, bytes, 0x00020000); }
DI v4u ld16(rsrc_t rs, int voff, int soff) { return __builtin_amdgcn_raw_buffer_load_b128(rs, voff, soff, 0); }
DI v2u ld8(rsrc_t rs, int voff, int soff) { return __builtin_amdgcn_raw_buffer_load_b64(rs, voff, soff, 0); }
DI void st16(v4u v, rsrc_t rs, int voff, int soff) { __builtin_amdgcn_raw_buffer_store_b128(v, rs, voff + soff, 0, 0); }
DI void st8(v2u v, rsrc_t rs, int voff, int soff) { __builtin_amdgcn_raw_buffer_store_b64(v, rs, voff, soff, 0); }
DI void dma16(rsrc_t rs, int voff, int soff, int lds) {
  unsigned keep;
  asm volatile("s_mov_b32 %0, m0\n\ts_mov_b32 m0, %1\n\ts_nop 0\n\tbuffer_load_dwordx4 %2, %3, %4 offen lds\n\ts_mov_b32 m0, %0"
               : "=&s"(keep) : "s"(lds), "v"(voff), "s"(rs), "s"(soff) : "memory");
}
#define WAITV(n) asm volatile("s_waitcnt vmcnt(" #n ")" ::: "memory")
#define BAR() asm volatile("s_waitcnt lgkmcnt(0)\n\ts_barrier" ::: "memory")
DI v4u lds16(LAS char* p, int off) { return *(LAS v4u*)(p + off); }
DI v2u ldstr(LAS char* p, int off) { return __builtin_bit_cast(v2u, __builtin_amdgcn_ds_read_tr16_b64_v4bf16((LAS bf16x4*)(p + off))); }
DI v4u cat(v2u a, v2u b) { return v4u{a.x, a.y, b.x, b.y}; }
template <int SS> DI v4u packfrag(const f32x16& a) {
  return v4u{pk2(a[8 * SS], a[8 * SS + 1]), pk2(a[8 * SS + 2], a[8 * SS + 3]), pk2(a[8 * SS + 4], a[8 * SS + 5]), pk2(a[8 * SS + 6], a[8 * SS + 7])};
}
constexpr unsigned ONES = 0x3F803F80u;
constexpr float LOG2E = 1.4426950408889634f;
DI float clamp01(float x) { return __builtin_amdgcn_fmed3f(x, 0.f, 1.f); }
DI float phi6(float tp) { return fmaxf(tp, 1e-6f) + clamp01(__builtin_amdgcn_exp2f(tp * LOG2E)); }

constexpr int L1_W = 0, L1_X = 86016, L1_MISC = L1_X + 65536;
constexpr int L1_BYTES = L1_MISC + 4096;

__global__ __launch_bounds__(256, 1) void k1(const u16* __restrict__ x, const u16* __restrict__ wqkv, const u16* __restrict__ wo,
                                             const u16* __restrict__ w1, const u16* __restrict__ w2, unsigned char* __restrict__ ws) {
  extern __shared__ __attribute__((aligned(16))) char smem_[];
  LAS char* L = (LAS char*)smem_;
  const int lb = (int)(size_t)L;
  STAMP_DECL; STAMP(0);
  const int b = blockIdx.x, xcd = b & 7, idx = b >> 3, tm = 2 * xcd + (idx >> 4), head = idx & 15, odd = head & 1;
  const int lane = threadIdx.x & 63, w = __builtin_amdgcn_readfirstlane(threadIdx.x >> 6), hh = lane >> 5, r = lane & 31;
  const rsrc_t rsX = mkrs(x, S * D * 2), rsW = mkrs(wqkv, D * 3 * D * 2), rsS = mkrs(ws, WS_END);
  const int row0 = tm * 128 + w * 32;
  const int voffW = (lane >> 2) * 2688 + (lane & 3) * 16 + ((56 * head) & ~15);
  const int voffX0 = (row0 + (lane >> 3)) * 896 + (((lane & 7) ^ (lane >> 4)) * 16);
  const int voffX1 = (row0 + 8 + (lane >> 3)) * 896 + (((lane & 7) ^ (4 + (lane >> 4))) * 16);
  const int xs = L1_X + w * 16384;
#pragma unroll
  for (int c = 0; c < 4; c++) {
    dma16(rsW, voffW, 896 + (4 * c + w) * 43008, lb + L1_W + 28672 + (4 * c + w) * 1024);
    dma16(rsW, voffW, 1792 + (4 * c + w) * 43008, lb + L1_W + 57344 + (4 * c + w) * 1024);
#pragma unroll
    for (int i = 0; i < 4; i++) dma16(rsX, (i & 1) ? voffX1 : voffX0, (i >> 1) * 14336 + c * 128, lb + xs + c * 4096 + i * 1024);
  }
#pragma unroll
  for (int c = 4; c < 7; c++) {
    dma16(rsW, voffW, 896 + (4 * c + w) * 43008, lb + L1_W + 28672 + (4 * c + w) * 1024);
    dma16(rsW, voffW, 1792 + (4 * c + w) * 43008, lb + L1_W + 57344 + (4 * c + w) * 1024);
  }
  v4u xr[12];
#pragma unroll
  for (int c = 4; c < 7; c++)
#pragma unroll
    for (int i = 0; i < 4; i++) xr[(c - 4) * 4 + i] = ld16(rsX, (i & 1) ? voffX1 : voffX0, (i >> 1) * 14336 + c * 128);
  __builtin_amdgcn_sched_barrier(0);
#pragma unroll
  for (int i = 0; i < 7; i++) dma16(rsW, voffW, (4 * i + w) * 43008, lb + L1_W + (4 * i + w) * 1024);
  int hu = 4 * b + w;
  if (hu >= 896) hu -= 128;
  const u16* psrc; int pld, prow, pcol, pdst0, pdst1, pkind;
  if (hu < 392) { pkind = 0; psrc = w1; pld = F * 2; const int kc = hu / 28, cb = hu % 28; prow = 32 * kc; pcol = 128 * cb;
    pdst0 = WS_W1T + ((2 * cb) * 28 + 2 * kc) * 1024; pdst1 = pdst0 + 28 * 1024; }
  else if (hu < 784) { pkind = 1; psrc = w2; pld = D * 2; const int u = hu - 392, kc = u / 7, cb = u % 7; prow = 32 * kc; pcol = 128 * cb;
    pdst0 = WS_W2T + ((2 * cb) * 112 + 2 * kc) * 1024; pdst1 = pdst0 + 112 * 1024; }
  else { pkind = 2; psrc = wo; pld = D * 2; const int u = hu - 784, hd = u / 7, cb = u % 7; prow = 28 * hd - 4 * (hd & 1); pcol = 128 * cb;
    pdst0 = WS_WOT + ((2 * cb) * 32 + 2 * hd) * 1024; pdst1 = pdst0 + 32 * 1024; }
  const rsrc_t rsP = mkrs(psrc, pkind == 2 ? D * D * 2 : D * F * 2);
  v4u pt[4];
#pragma unroll
  for (int i = 0; i < 4; i++) pt[i] = ld16(rsP, (prow + 8 * i + (lane >> 3)) * pld + pcol + (lane & 7) * 16, 0);
  __builtin_amdgcn_sched_barrier(0);

  STAMP(1);
  const int trb = ((lane >> 5) * 8 + ((lane & 15) >> 2)) * 64 + ((lane >> 4) & 1) * 32 + (lane & 3) * 8;
  const int xsw = (r >> 1) & 7;
  f32x16 ak = {}, av = {};
  f32x4 sq = {}, sm = {}, ck = {}, cv = {};
  v4u xf[28];
#pragma unroll
  for (int c = 0; c < 7; c++) {
    if (c == 0) { WAITV(47); BAR(); STAMP(2); }
    if (c == 1) { WAITV(41); BAR(); }
    if (c == 2) { WAITV(35); BAR(); STAMP(3); }
    if (c == 3) { WAITV(29); BAR(); }
    if (c == 4) { WAITV(23); BAR(); STAMP(4); }
    if (c >= 3 && c < 6) {
#pragma unroll
      for (int i = 0; i < 4; i++) *(LAS v4u*)(L + xs + (c - 3) * 4096 + i * 1024 + lane * 16) = xr[(c - 3) * 4 + i];
    }
#pragma unroll
    for (int s4 = 0; s4 < 4; s4++) {
      const int s = 4 * c + s4;
      xf[s] = lds16(L, xs + (c & 3) * 4096 + r * 128 + (((2 * s4 + hh) ^ xsw) * 16));
      const v4u wk = cat(ldstr(L, L1_W + 28672 + s * 1024 + trb), ldstr(L, L1_W + 28672 + s * 1024 + 256 + trb));
      const v4u wv = cat(ldstr(L, L1_W + 57344 + s * 1024 + trb), ldstr(L, L1_W + 57344 + s * 1024 + 256 + trb));
      ak = mfma32(xf[s], wk, ak);
      av = mfma32(xf[s], wv, av);
      sq = mfma4(xf[s].x, xf[s].y, xf[s].x, xf[s].y, sq);
      sm = mfma4(ONES, ONES, xf[s].x, xf[s].y, sm);
      sq = mfma4(xf[s].z, xf[s].w, xf[s].z, xf[s].w, sq);
      sm = mfma4(ONES, ONES, xf[s].z, xf[s].w, sm);
      if (s4 == w) {
        ck = mfma4(ONES, ONES, wk.x, wk.y, ck); cv = mfma4(ONES, ONES, wv.x, wv.y, cv);
        ck = mfma4(ONES, ONES, wk.z, wk.w, ck); cv = mfma4(ONES, ONES, wv.z, wv.w, cv);
      }
    }
  }
  STAMP(5);
  float ssq = (lane & 2) ? ((lane & 1) ? sq[3] : sq[2]) : ((lane & 1) ? sq[1] : sq[0]);
  ssq = halfsum(ssq);
  const float mean = halfsum(sm[0]) * (1.f / D);
  const float var = fmaxf(ssq * (1.f / D) - mean * mean, 0.f);
  const float rs = __builtin_amdgcn_rsqf(var + 1e-5f), mrs = mean * rs;
  LAS float* stt = (LAS float*)(L + L1_MISC) + w * 64;
  LAS float* csx = (LAS float*)(L + L1_MISC + 1024);
  {
    const float cks = halfsum(ck[0]), cvs = halfsum(cv[0]);
    stt[lane] = hh ? mrs : rs;
    csx[w * 64 + lane] = hh ? cvs : cks;
  }
  WAITV(4);
  BAR();
  STAMP(6);
  const float csk = (csx[r] + csx[64 + r]) + (csx[128 + r] + csx[192 + r]);
  const float csv = (csx[32 + r] + csx[96 + r]) + (csx[160 + r] + csx[224 + r]);
#pragma unroll
  for (int g = 0; g < 4; g++) {
    const f32x4 r4 = *(LAS f32x4*)(stt + 8 * g + 4 * hh), m4 = *(LAS f32x4*)(stt + 32 + 8 * g + 4 * hh);
#pragma unroll
    for (int j = 0; j < 4; j++) {
      ak[4 * g + j] = phi6(fmaf(ak[4 * g + j], r4[j], fmaf(-m4[j], csk, 1e-6f)));
      av[4 * g + j] = fmaf(av[4 * g + j], r4[j], -m4[j] * csv);
    }
  }
  {
    v4u p0 = packfrag<0>(ak), p1 = packfrag<1>(ak), v0 = packfrag<0>(av), v1 = packfrag<1>(av);
    if (r == (odd ? 0 : 28)) { v0 = v4u{ONES, ONES, ONES, ONES}; v1 = v0; }
    f32x16 kv = {};
    kv = mfma32(p0, v0, kv);
    kv = mfma32(p1, v1, kv);
    LAS char* red = L + L1_X;
#pragma unroll
    for (int q = 0; q < 4; q++) *(LAS f32x4*)(red + (q * 4 + w) * 1024 + lane * 16) = f32x4{kv[4 * q], kv[4 * q + 1], kv[4 * q + 2], kv[4 * q + 3]};
    BAR();
    f32x4 t = *(LAS f32x4*)(red + (w * 4 + 0) * 1024 + lane * 16) + *(LAS f32x4*)(red + (w * 4 + 1) * 1024 + lane * 16);
    t += *(LAS f32x4*)(red + (w * 4 + 2) * 1024 + lane * 16) + *(LAS f32x4*)(red + (w * 4 + 3) * 1024 + lane * 16);
    st16(__builtin_bit_cast(v4u, t), rsS, lane * 16, WS_KVP + ((head * 16 + tm) * 4 + w) * 1024);
  }
  STAMP(7);
  f32x16 aq = {};
  f32x4 cq = {};
#pragma unroll
  for (int s = 0; s < 28; s++) {
    const v4u wq = cat(ldstr(L, L1_W + s * 1024 + trb), ldstr(L, L1_W + s * 1024 + 256 + trb));
    aq = mfma32(wq, xf[s], aq);
    cq = mfma4(ONES, ONES, wq.x, wq.y, cq);
    cq = mfma4(ONES, ONES, wq.z, wq.w, cq);
  }
  {
    LAS float* cqx = (LAS float*)(L + L1_MISC + 2048) + w * 64;
    cqx[lane] = halfsum(cq[0]);
#pragma unroll
    for (int g = 0; g < 4; g++) {
      const f32x4 c4 = *(LAS f32x4*)(cqx + 8 * g + 4 * hh);
#pragma unroll
      for (int j = 0; j < 4; j++) aq[4 * g + j] = phi6(fmaf(aq[4 * g + j], rs, fmaf(-mrs, c4[j], 1e-6f)));
    }
    const int tile = tm * 4 + w;
    st16(packfrag<0>(aq), rsS, lane * 16, WS_PQ + (tile * 32 + 2 * head) * 1024);
    st16(packfrag<1>(aq), rsS, lane * 16, WS_PQ + (tile * 32 + 2 * head + 1) * 1024);
  }
  STAMP(8);
  {
    LAS char* stg = L + L1_X + 16384 + w * 4096;
#pragma unroll
    for (int i = 0; i < 4; i++) {
      v4u v = pt[i];
      if (pkind == 2) {
        const int e = 8 * i + (lane >> 3);
        const bool hodd = (prow % 28) != 0 || false;
        const bool pad = (((hu - 784) / 7) & 1) ? (e < 4) : (e >= 28);
        (void)hodd;
        if (pad) v = v4u{0, 0, 0, 0};
      }
      *(LAS v4u*)(stg + i * 1024 + lane * 16) = v;
    }
    const int tb = ((lane >> 5) * 4 + ((lane & 15) >> 2)) * 128 + ((lane >> 4) & 1) * 32 + (lane & 3) * 8;
    f32x4 c0 = {}, c1 = {};
#pragma unroll
    for (int s = 0; s < 2; s++) {
      const v4u f0 = cat(ldstr(stg, s * 2048 + tb), ldstr(stg, s * 2048 + 1024 + tb));
      const v4u f1 = cat(ldstr(stg, s * 2048 + 64 + tb), ldstr(stg, s * 2048 + 1024 + 64 + tb));
      st16(f0, rsS, lane * 16, pdst0 + s * 1024);
      st16(f1, rsS, lane * 16, pdst1 + s * 1024);
      c0 = mfma4(ONES, ONES, f0.x, f0.y, c0); c0 = mfma4(ONES, ONES, f0.z, f0.w, c0);
      c1 = mfma4(ONES, ONES, f1.x, f1.y, c1); c1 = mfma4(ONES, ONES, f1.z, f1.w, c1);
    }
    const float s0 = halfsum(c0[0]), s1 = halfsum(c1[0]);
    if (pkind == 0) __builtin_amdgcn_raw_buffer_store_b32(__builtin_bit_cast(unsigned, hh ? s1 : s0), rsS, lane * 4, WS_C1P + (prow / 32) * (F * 4) + pcol * 2, 0);
  }
  STAMP(9); WAITV(0); STAMP(10); STAMP_OUT(0, 10);
}

__global__ __launch_bounds__(256, 1) void k1b(unsigned char* __restrict__ ws) {
  __shared__ __attribute__((aligned(16))) char smem_[2048];
  LAS char* L = (LAS char*)smem_;
  const int b = blockIdx.x, xcd = b & 7, idx = b >> 3, tm = 2 * xcd + (idx >> 4), head = idx & 15, odd = head & 1;
  const int lane = threadIdx.x & 63, w = __builtin_amdgcn_readfirstlane(threadIdx.x >> 6), hh = lane >> 5;
  const rsrc_t rsS = mkrs(ws, WS_END);
  STAMP_DECL; STAMP(11);
  const int tile = tm * 4 + w;
  v4u p[16];
#pragma unroll
  for (int i = 0; i < 16; i++) p[i] = ld16(rsS, lane * 16, WS_KVP + ((head * 16 + i) * 4 + w) * 1024);
  const v4u q0 = ld16(rsS, lane * 16, WS_PQ + (tile * 32 + 2 * head) * 1024), q1 = ld16(rsS, lane * 16, WS_PQ + (tile * 32 + 2 * head + 1) * 1024);
  __builtin_amdgcn_sched_barrier(0);
  f32x4 t = __builtin_bit_cast(f32x4, p[0]);
#pragma unroll
  for (int i = 1; i < 16; i++) t += __builtin_bit_cast(f32x4, p[i]);
  v2u o2 = {pk2(t[0], t[1]), pk2(t[2], t[3])};
  if (odd ? (w == 0 && hh == 0) : (w == 3 && hh == 1)) o2 = v2u{0, 0};
  *(LAS v2u*)(L + (w >> 1) * 1024 + lane * 16 + (w & 1) * 8) = o2;
  BAR();
  const v4u k0 = lds16(L, lane * 16), k1f = lds16(L, 1024 + lane * 16);
  f32x16 o = {};
  o = mfma32(k0, q0, o);
  o = mfma32(k1f, q1, o);
  const unsigned tu = __builtin_bit_cast(unsigned, odd ? o[0] : o[12]);
  unsigned tu2 = tu;
  asm("" : "+v"(tu2));
  auto sw = __builtin_amdgcn_permlane32_swap(tu, tu2, false, false);
  const float den = __builtin_bit_cast(float, odd ? sw[0] : sw[1]);
  const float inv = __builtin_amdgcn_rcpf(den + 1e-6f);
#pragma unroll
  for (int i = 0; i < 16; i++) o[i] *= inv;
  st16(packfrag<0>(o), rsS, lane * 16, WS_OF + (tile * 32 + 2 * head) * 1024);
  st16(packfrag<1>(o), rsS, lane * 16, WS_OF + (tile * 32 + 2 * head + 1) * 1024);
  STAMP(12); WAITV(0); STAMP(13); STAMP_OUT(11, 13);
}

template <int NKS, int WIN, int WSTR, int ASTR>
DI f32x16 gemm64(LAS char* L, rsrc_t rsS, int lane, int w, int wbase, int abase) {
  v4u fw0[WIN], fw1[WIN], fa0[WIN], fa1[WIN];
  const int vo = lane * 16;
#pragma unroll
  for (int i = 0; i < WIN; i++) {
    fw0[i] = ld16(rsS, vo, wbase + i * 1024); fa0[i] = ld16(rsS, vo, abase + i * 1024);
    fw1[i] = ld16(rsS, vo, wbase + WSTR + i * 1024); fa1[i] = ld16(rsS, vo, abase + ASTR + i * 1024);
  }
  __builtin_amdgcn_sched_barrier(0);
  f32x16 a00 = {}, a01 = {}, a10 = {}, a11 = {};
#pragma unroll
  for (int i = 0; i < NKS; i++) {
    const v4u w0 = fw0[i % WIN], w1 = fw1[i % WIN], x0 = fa0[i % WIN], x1 = fa1[i % WIN];
    a00 = mfma32(w0, x0, a00);
    a10 = mfma32(w1, x0, a10);
    a01 = mfma32(w0, x1, a01);
    a11 = mfma32(w1, x1, a11);
    if (i + WIN < NKS) {
      fw0[i % WIN] = ld16(rsS, vo, wbase + (i + WIN) * 1024); fa0[i % WIN] = ld16(rsS, vo, abase + (i + WIN) * 1024);
      fw1[i % WIN] = ld16(rsS, vo, wbase + WSTR + (i + WIN) * 1024); fa1[i % WIN] = ld16(rsS, vo, abase + ASTR + (i + WIN) * 1024);
    }
    __builtin_amdgcn_sched_barrier(0);
  }
  f32x16 own;
#define PUT(T, A) \
  if (w == T) own = A; \
  else { const int sl = w - (w > T ? 1 : 0); _Pragma("unroll") for (int q = 0; q < 4; q++) \
    *(LAS f32x4*)(L + ((T * 3 + sl) * 4 + q) * 1024 + vo) = f32x4{A[4 * q], A[4 * q + 1], A[4 * q + 2], A[4 * q + 3]}; }
  PUT(0, a00) PUT(1, a10) PUT(2, a01) PUT(3, a11)
#undef PUT
  BAR();
#pragma unroll
  for (int sl = 0; sl < 3; sl++)
#pragma unroll
    for (int q = 0; q < 4; q++) {
      const f32x4 v = *(LAS f32x4*)(L + ((w * 3 + sl) * 4 + q) * 1024 + vo);
#pragma unroll
      for (int j = 0; j < 4; j++) own[4 * q + j] += v[j];
    }
  return own;
}

__global__ __launch_bounds__(256, 1) void k2(const u16* __restrict__ x, const u16* __restrict__ b1, unsigned char* __restrict__ ws) {
  __shared__ __attribute__((aligned(16))) char smem_[49152];
  LAS char* L = (LAS char*)smem_;
  const int b = blockIdx.x, xcd = b & 7, idx = b >> 3;
  const int lane = threadIdx.x & 63, w = __builtin_amdgcn_readfirstlane(threadIdx.x >> 6), hh = lane >> 5, r = lane & 31;
  const rsrc_t rsS = mkrs(ws, WS_END);
  if (idx >= 28) {
    const int f = ((idx - 28) * 8 + xcd) * 256 + threadIdx.x;
    if (f < F) {
      const float* cp = (const float*)(ws + WS_C1P);
      float v[14];
#pragma unroll
      for (int i = 0; i < 14; i++) v[i] = cp[i * F + f];
      float s = 0.f;
#pragma unroll
      for (int i = 0; i < 14; i++) s += v[i];
      const int ft = f >> 5, rr = f & 31, h2 = (rr >> 2) & 1, reg = (rr & 3) + 4 * (rr >> 3);
      float* o = (float*)(ws + WS_C1B) + ft * 64 + h2 * 16 + reg;
      o[0] = s;
      o[32] = bflo(b1[f]);
    }
    return;
  }
  const int g = 4 * xcd + idx / 7, j = idx % 7;
  const int tile = 2 * g + (w >> 1), ct = 2 * j + (w & 1);
  STAMP_DECL; STAMP(14);
  const rsrc_t rsX = mkrs(x, S * D * 2);
  v2u xr[4];
#pragma unroll
  for (int q = 0; q < 4; q++) xr[q] = ld8(rsX, (tile * 32 + r) * 896 + hh * 8, ct * 64 + q * 16);
  f32x16 a = gemm64<8, 8, 32 * 1024, 32 * 1024>(L, rsS, lane, w, WS_WOT + (2 * j * 32 + 8 * w) * 1024, WS_OF + (2 * g * 32 + 8 * w) * 1024);
  STAMP(15);
  unsigned hp[8];
  float s1 = 0.f, s2 = 0.f;
#pragma unroll
  for (int q = 0; q < 4; q++) {
    hp[2 * q] = pk2(a[4 * q] + bflo(xr[q].x), a[4 * q + 1] + bfhi(xr[q].x));
    hp[2 * q + 1] = pk2(a[4 * q + 2] + bflo(xr[q].y), a[4 * q + 3] + bfhi(xr[q].y));
  }
  st16(v4u{hp[0], hp[1], hp[2], hp[3]}, rsS, lane * 16, WS_HF + (tile * 28 + 2 * ct) * 1024);
  st16(v4u{hp[4], hp[5], hp[6], hp[7]}, rsS, lane * 16, WS_HF + (tile * 28 + 2 * ct + 1) * 1024);
#pragma unroll
  for (int q = 0; q < 8; q++) { const float lo = bflo(hp[q]), hi = bfhi(hp[q]); s1 += lo + hi; s2 = fmaf(lo, lo, fmaf(hi, hi, s2)); }
  s1 = halfsum(s1); s2 = halfsum(s2);
  if (hh == 0) st8(v2u{__builtin_bit_cast(unsigned, s1), __builtin_bit_cast(unsigned, s2)}, rsS, r * 8, WS_HST + (tile * 14 + ct) * 256);
  WAITV(0); STAMP(16); STAMP_OUT(14, 16);
}

DI float gelu_f(float y) {
  constexpr float C1 = -2.f * 0.7978845608028654f * LOG2E, C2 = C1 * 0.044715f;
  const float e = __builtin_amdgcn_exp2f(y * fmaf(y * y, C2, C1));
  return y * __builtin_amdgcn_rcpf(1.f + e);
}
constexpr int L3_ST = 114688, L3_BYTES = L3_ST + 1024;
__global__ __launch_bounds__(256, 1) void k3(unsigned char* __restrict__ ws) {
  extern __shared__ __attribute__((aligned(16))) char smem_[];
  LAS char* L = (LAS char*)smem_;
  const int b = blockIdx.x, xcd = b & 7, idx = b >> 3;
  if (idx >= 28) return;
  const int lane = threadIdx.x & 63, w = __builtin_amdgcn_readfirstlane(threadIdx.x >> 6), hh = lane >> 5, r = lane & 31;
  const rsrc_t rsS = mkrs(ws, WS_END);
  const int tm = 2 * xcd + idx / 14, ft = 4 * (idx % 14) + w, vo = lane * 16;
  STAMP_DECL; STAMP(17);
  v4u hr[4][7], wf[28];
  v2u st[7];
  f32x4 cc[4], bb[4];
#pragma unroll
  for (int i = 0; i < 7; i++) hr[0][i] = ld16(rsS, vo, WS_HF + ((tm * 4) * 28 + 7 * w + i) * 1024);
#pragma unroll
  for (int i = 0; i < 7; i++) st[i] = ld8(rsS, lane * 8, WS_HST + ((tm * 4 + w) * 14 + 2 * i) * 256);
#pragma unroll
  for (int i = 0; i < 28; i++) wf[i] = ld16(rsS, vo, WS_W1T + (ft * 28 + i) * 1024);
#pragma unroll
  for (int q = 0; q < 4; q++) {
    cc[q] = __builtin_bit_cast(f32x4, ld16(rsS, hh * 64, WS_C1B + ft * 256 + q * 16));
    bb[q] = __builtin_bit_cast(f32x4, ld16(rsS, hh * 64, WS_C1B + ft * 256 + 128 + q * 16));
  }
#pragma unroll
  for (int i = 0; i < 7; i++) hr[1][i] = ld16(rsS, vo, WS_HF + ((tm * 4 + 1) * 28 + 7 * w + i) * 1024);
  __builtin_amdgcn_sched_barrier(0);
#pragma unroll
  for (int i = 0; i < 7; i++) *(LAS v4u*)(L + (7 * w + i) * 1024 + vo) = hr[0][i];
  {
    float s1 = 0.f, s2 = 0.f;
#pragma unroll
    for (int i = 0; i < 7; i++) { s1 += asf(st[i].x); s2 += asf(st[i].y); }
    s1 = halfsum(s1); s2 = halfsum(s2);
    const float mean = s1 * (1.f / D), var = fmaxf(s2 * (1.f / D) - mean * mean, 0.f);
    const float rs = __builtin_amdgcn_rsqf(var + 1e-5f);
    if (hh == 0) *(LAS f32x2*)(L + L3_ST + w * 256 + r * 8) = f32x2{rs, -mean * rs};
  }
#pragma unroll
  for (int i = 0; i < 7; i++) hr[2][i] = ld16(rsS, vo, WS_HF + ((tm * 4 + 2) * 28 + 7 * w + i) * 1024);
#pragma unroll
  for (int i = 0; i < 7; i++) hr[3][i] = ld16(rsS, vo, WS_HF + ((tm * 4 + 3) * 28 + 7 * w + i) * 1024);
  f32x16 acc[2];
#pragma unroll
  for (int t = 0; t <= 4; t++) {
    if (t < 4) {
      BAR();
      STAMP(18 + t);
      acc[t & 1] = f32x16{};
#pragma unroll
      for (int i = 0; i < 28; i++) acc[t & 1] = mfma32(wf[i], lds16(L, (t * 28 + i) * 1024 + vo), acc[t & 1]);
    }
    if (t > 0) {
      const f32x2 sv = *(LAS f32x2*)(L + L3_ST + (t - 1) * 256 + r * 8);
      f32x16& a = acc[(t - 1) & 1];
#pragma unroll
      for (int q = 0; q < 4; q++)
#pragma unroll
        for (int j = 0; j < 4; j++) a[4 * q + j] = gelu_f(fmaf(a[4 * q + j], sv.x, fmaf(sv.y, cc[q][j], bb[q][j])));
      st16(packfrag<0>(a), rsS, vo, WS_H1F + ((tm * 4 + t - 1) * 112 + 2 * ft) * 1024);
      st16(packfrag<1>(a), rsS, vo, WS_H1F + ((tm * 4 + t - 1) * 112 + 2 * ft + 1) * 1024);
    }
    if (t < 3) {
#pragma unroll
      for (int i = 0; i < 7; i++) *(LAS v4u*)(L + ((t + 1) * 28 + 7 * w + i) * 1024 + vo) = hr[t + 1][i];
    }
    if (t == 3) STAMP(22);
  }
  STAMP(23); WAITV(0); STAMP(24); STAMP_OUT(17, 24);
}

__global__ __launch_bounds__(256, 1) void k4(const u16* __restrict__ b2, u16* __restrict__ out, unsigned char* __restrict__ ws) {
  __shared__ __attribute__((aligned(16))) char smem_[49152];
  LAS char* L = (LAS char*)smem_;
  const int b = blockIdx.x, xcd = b & 7, idx = b >> 3;
  if (idx >= 28) return;
  const int lane = threadIdx.x & 63, w = __builtin_amdgcn_readfirstlane(threadIdx.x >> 6), hh = lane >> 5, r = lane & 31;
  const rsrc_t rsS = mkrs(ws, WS_END), rsB = mkrs(b2, D * 2), rsO = mkrs(out, S * D * 2);
  const int g = 4 * xcd + idx / 7, j = idx % 7;
  const int tile = 2 * g + (w >> 1), ct = 2 * j + (w & 1);
  STAMP_DECL; STAMP(25);
  const v4u h0 = ld16(rsS, lane * 16, WS_HF + (tile * 28 + 2 * ct) * 1024), h1 = ld16(rsS, lane * 16, WS_HF + (tile * 28 + 2 * ct + 1) * 1024);
  v2u br[4];
#pragma unroll
  for (int q = 0; q < 4; q++) br[q] = ld8(rsB, hh * 8, ct * 64 + q * 16);
  f32x16 a = gemm64<28, 10, 112 * 1024, 112 * 1024>(L, rsS, lane, w, WS_W2T + (2 * j * 112 + 28 * w) * 1024, WS_H1F + (2 * g * 112 + 28 * w) * 1024);
  STAMP(26);
  const unsigned hp[8] = {h0.x, h0.y, h0.z, h0.w, h1.x, h1.y, h1.z, h1.w};
#pragma unroll
  for (int q = 0; q < 4; q++) {
    v2u o;
    o.x = pk2(a[4 * q] + bflo(br[q].x) + bflo(hp[2 * q]), a[4 * q + 1] + bfhi(br[q].x) + bfhi(hp[2 * q]));
    o.y = pk2(a[4 * q + 2] + bflo(br[q].y) + bflo(hp[2 * q + 1]), a[4 * q + 3] + bfhi(br[q].y) + bfhi(hp[2 * q + 1]));
    st8(o, rsO, (tile * 32 + r) * 896 + hh * 8, ct * 64 + q * 16);
  }
  WAITV(0); STAMP(27); STAMP_OUT(25, 27);
}

extern "C" void kernel_launch(void* const* d_in, const int* in_sizes, int n_in, void* d_out, int out_size, void* d_ws, size_t ws_size,
                              hipStream_t stream) {
  const u16* x = (const u16*)d_in[0];
  const u16* wqkv = (const u16*)d_in[1];
  const u16* wo = (const u16*)d_in[2];
  const u16* w1 = (const u16*)d_in[4];
  const u16* b1 = (const u16*)d_in[5];
  const u16* w2 = (const u16*)d_in[6];
  const u16* b2 = (const u16*)d_in[7];
  unsigned char* ws = (unsigned char*)d_ws;
  (void)hipFuncSetAttribute((const void*)k1, hipFuncAttributeMaxDynamicSharedMemorySize, L1_BYTES);
  (void)hipFuncSetAttribute((const void*)k3, hipFuncAttributeMaxDynamicSharedMemorySize, L3_BYTES);
  k1<<<256, 256, L1_BYTES, stream>>>(x, wqkv, wo, w1, w2, ws);
  k1b<<<256, 256, 0, stream>>>(ws);
  k2<<<256, 256, 0, stream>>>(x, b1, ws);
  k3<<<256, 256, L3_BYTES, stream>>>(ws);
  k4<<<256, 256, 0, stream>>>(b2, (u16*)d_out, ws);
}
```

```cpp
#define STAMPS
#include <hip/hip_runtime.h>
#include <hip/hip_bf16.h>
#include <stdint.h>

typedef unsigned short u16;
typedef unsigned v4u __attribute__((ext_vector_type(4)));
typedef unsigned v2u __attribute__((ext_vector_type(2)));
typedef float f32x2 __attribute__((ext_vector_type(2)));
typedef float f32x4 __attribute__((ext_vector_type(4)));
typedef float f32x16 __attribute__((ext_vector_type(16)));
typedef __bf16 bf16x8 __attribute__((ext_vector_type(8)));
typedef __bf16 bf16x4 __attribute__((ext_vector_type(4)));
typedef __bf16 bf16x2 __attribute__((ext_vector_type(2)));
typedef short s16x4 __attribute__((ext_vector_type(4)));
#define LAS __attribute__((address_space(3)))
#define DI __device__ __forceinline__
typedef __amdgpu_buffer_rsrc_t rsrc_t;

constexpr int S = 2048, D = 448, F = 1792;
constexpr int WS_CTRL = 0;
constexpr int WS_KVP = 65536;
constexpr int WS_PQ = WS_KVP + 16 * 16 * 4096;
constexpr int WS_OF = WS_PQ + 64 * 32 * 1024;
constexpr int WS_HF = WS_OF + 64 * 32 * 1024;
constexpr int WS_HST = WS_HF + 64 * 28 * 1024;
constexpr int WS_H1F = WS_HST + 64 * 14 * 256;
constexpr int WS_WOT = WS_H1F + 64 * 112 * 1024;
constexpr int WS_W1T = WS_WOT + 14 * 32 * 1024;
constexpr int WS_W2T = WS_W1T + 56 * 28 * 1024;
constexpr int WS_C1P = WS_W2T + 14 * 112 * 1024;
constexpr int WS_C1B = WS_C1P + 14 * 1792 * 4;
constexpr int WS_END = WS_C1B + 56 * 256;
constexpr int WS_STAMP = WS_END + 4096;
constexpr int NSTAMP = 32;
#define STAMP(i) do { __builtin_amdgcn_sched_barrier(0); ts_[i] = __builtin_amdgcn_s_memrealtime(); __builtin_amdgcn_sched_barrier(0); } while (0)
#define STAMP_DECL unsigned long long ts_[32]
#define STAMP_OUT(lo, hi) do { if (threadIdx.x == 0) { _Pragma("unroll") for (int i_ = lo; i_ <= hi; i_++) ((unsigned long long*)(ws + WS_STAMP))[blockIdx.x * 32 + i_] = ts_[i_]; } } while (0)

DI f32x16 mfma32(v4u a, v4u b, f32x16 c) {
  return __builtin_amdgcn_mfma_f32_32x32x16_bf16(__builtin_bit_cast(bf16x8, a), __builtin_bit_cast(bf16x8, b), c, 0, 0, 0);
}
DI f32x4 mfma4(unsigned a0, unsigned a1, unsigned b0, unsigned b1, f32x4 c) {
  v2u a = {a0, a1}, b = {b0, b1};
  return __builtin_amdgcn_mfma_f32_4x4x4bf16_1k(__builtin_bit_cast(s16x4, a), __builtin_bit_cast(s16x4, b), c, 0, 0, 0);
}
DI unsigned pk2(float lo, float hi) { bf16x2 v = {(__bf16)lo, (__bf16)hi}; return __builtin_bit_cast(unsigned, v); }
DI float asf(unsigned u) { return __builtin_bit_cast(float, u); }
DI float bflo(unsigned u) { return __builtin_bit_cast(float, u << 16); }
DI float bfhi(unsigned u) { return __builtin_bit_cast(float, u & 0xffff0000u); }
DI float halfsum(float a) {
  unsigned u = __builtin_bit_cast(unsigned, a), u2 = u;
  asm("" : "+v"(u2));
  auto r = __builtin_amdgcn_permlane32_swap(u, u2, false, false);
  const unsigned r0 = r[0], r1 = r[1];
  return __builtin_bit_cast(float, r0) + __builtin_bit_cast(float, r1);
}
DI rsrc_t mkrs(const void* p, int bytes) { return __builtin_amdgcn_make_buffer_rsrc((void*)p, 0, bytes, 0x00020000); }
DI v4u ld16(rsrc_t rs, int voff, int soff) { return __builtin_amdgcn_raw_buffer_load_b128(rs, voff, soff, 0); }
DI v2u ld8(rsrc_t rs, int voff, int soff) { return __builtin_amdgcn_raw_buffer_load_b64(rs, voff, soff, 0); }
DI void st16(v4u v, rsrc_t rs, int voff, int soff) { __builtin_amdgcn_raw_buffer_store_b128(v, rs, voff + soff, 0, 0); }
DI void st8(v2u v, rsrc_t rs, int voff, int soff) { __builtin_amdgcn_raw_buffer_store_b64(v, rs, voff, soff, 0); }
DI void dma16(rsrc_t rs, int voff, int soff, int lds) {
  unsigned keep;
  asm volatile("s_mov_b32 %0, m0\n\ts_mov_b32 m0, %1\n\ts_nop 0\n\tbuffer_load_dwordx4 %2, %3, %4 offen lds\n\ts_mov_b32 m0, %0"
               : "=&s"(keep) : "s"(lds), "v"(voff), "s"(rs), "s"(soff) : "memory");
}
#define WAITV(n) asm volatile("s_waitcnt vmcnt(" #n ")" ::: "memory")
#define BAR() asm volatile("s_waitcnt lgkmcnt(0)\n\ts_barrier" ::: "memory")
DI v4u lds16(LAS char* p, int off) { return *(LAS v4u*)(p + off); }
DI v2u ldstr(LAS char* p, int off) { return __builtin_bit_cast(v2u, __builtin_amdgcn_ds_read_tr16_b64_v4bf16((LAS bf16x4*)(p + off))); }
DI v4u cat(v2u a, v2u b) { return v4u{a.x, a.y, b.x, b.y}; }
template <int SS> DI v4u packfrag(const f32x16& a) {
  return v4u{pk2(a[8 * SS], a[8 * SS + 1]), pk2(a[8 * SS + 2], a[8 * SS + 3]), pk2(a[8 * SS + 4], a[8 * SS + 5]), pk2(a[8 * SS + 6], a[8 * SS + 7])};
}
constexpr unsigned ONES = 0x3F803F80u;
constexpr float LOG2E = 1.4426950408889634f;
DI float clamp01(float x) { return __builtin_amdgcn_fmed3f(x, 0.f, 1.f); }
DI float phi6(float tp) { return fmaxf(tp, 1e-6f) + clamp01(__builtin_amdgcn_exp2f(tp * LOG2E)); }

constexpr int L1_W = 0, L1_X = 86016, L1_MISC = L1_X + 65536;
constexpr int L1_BYTES = L1_MISC + 4096;

__global__ __launch_bounds__(256, 1) void k1(const u16* __restrict__ x, const u16* __restrict__ wqkv, const u16* __restrict__ wo,
                                             const u16* __restrict__ w1, const u16* __restrict__ w2, unsigned char* __restrict__ ws) {
  extern __shared__ __attribute__((aligned(16))) char smem_[];
  LAS char* L = (LAS char*)smem_;
  const int lb = (int)(size_t)L;
  STAMP_DECL; STAMP(0);
  const int b = blockIdx.x, xcd = b & 7, idx = b >> 3, tm = 2 * xcd + (idx >> 4), head = idx & 15, odd = head & 1;
  const int lane = threadIdx.x & 63, w = __builtin_amdgcn_readfirstlane(threadIdx.x >> 6), hh = lane >> 5, r = lane & 31;
  const rsrc_t rsX = mkrs(x, S * D * 2), rsW = mkrs(wqkv, D * 3 * D * 2), rsS = mkrs(ws, WS_END);
  const int row0 = tm * 128 + w * 32;
  const int voffW = (lane >> 2) * 2688 + (lane & 3) * 16 + ((56 * head) & ~15);
  const int voffX0 = (row0 + (lane >> 3)) * 896 + (((lane & 7) ^ (lane >> 4)) * 16);
  const int voffX1 = (row0 + 8 + (lane >> 3)) * 896 + (((lane & 7) ^ (4 + (lane >> 4))) * 16);
  const int xs = L1_X + w * 16384;
#pragma unroll
  for (int c = 0; c < 4; c++) {
    dma16(rsW, voffW, 896 + (4 * c + w) * 43008, lb + L1_W + 28672 + (4 * c + w) * 1024);
    dma16(rsW, voffW, 1792 + (4 * c + w) * 43008, lb + L1_W + 57344 + (4 * c + w) * 1024);
#pragma unroll
    for (int i = 0; i < 4; i++) dma16(rsX, (i & 1) ? voffX1 : voffX0, (i >> 1) * 14336 + c * 128, lb + xs + c * 4096 + i * 1024);
  }
#pragma unroll
  for (int c = 4; c < 7; c++) {
    dma16(rsW, voffW, 896 + (4 * c + w) * 43008, lb + L1_W + 28672 + (4 * c + w) * 1024);
    dma16(rsW, voffW, 1792 + (4 * c + w) * 43008, lb + L1_W + 57344 + (4 * c + w) * 1024);
  }
  v4u xr[12];
#pragma unroll
  for (int c = 4; c < 7; c++)
#pragma unroll
    for (int i = 0; i < 4; i++) xr[(c - 4) * 4 + i] = ld16(rsX, (i & 1) ? voffX1 : voffX0, (i >> 1) * 14336 + c * 128);
  __builtin_amdgcn_sched_barrier(0);
#pragma unroll
  for (int i = 0; i < 7; i++) dma16(rsW, voffW, (4 * i + w) * 43008, lb + L1_W + (4 * i + w) * 1024);
  int hu = 4 * b + w;
  if (hu >= 896) hu -= 128;
  const u16* psrc; int pld, prow, pcol, pdst0, pdst1, pkind;
  if (hu < 392) { pkind = 0; psrc = w1; pld = F * 2; const int kc = hu / 28, cb = hu % 28; prow = 32 * kc; pcol = 128 * cb;
    pdst0 = WS_W1T + ((2 * cb) * 28 + 2 * kc) * 1024; pdst1 = pdst0 + 28 * 1024; }
  else if (hu < 784) { pkind = 1; psrc = w2; pld = D * 2; const int u = hu - 392, kc = u / 7, cb = u % 7; prow = 32 * kc; pcol = 128 * cb;
    pdst0 = WS_W2T + ((2 * cb) * 112 + 2 * kc) * 1024; pdst1 = pdst0 + 112 * 1024; }
  else { pkind = 2; psrc = wo; pld = D * 2; const int u = hu - 784, hd = u / 7, cb = u % 7; prow = 28 * hd - 4 * (hd & 1); pcol = 128 * cb;
    pdst0 = WS_WOT + ((2 * cb) * 32 + 2 * hd) * 1024; pdst1 = pdst0 + 32 * 1024; }
  const rsrc_t rsP = mkrs(psrc, pkind == 2 ? D * D * 2 : D * F * 2);
  v4u pt[4];
#pragma unroll
  for (int i = 0; i < 4; i++) pt[i] = ld16(rsP, (prow + 8 * i + (lane >> 3)) * pld + pcol + (lane & 7) * 16, 0);
  __builtin_amdgcn_sched_barrier(0);

  STAMP(1);
  const int trb = ((lane >> 5) * 8 + ((lane & 15) >> 2)) * 64 + ((lane >> 4) & 1) * 32 + (lane & 3) * 8;
  const int xsw = (r >> 1) & 7;
  f32x16 ak = {}, av = {};
  f32x4 sq = {}, sm = {}, ck = {}, cv = {};
  v4u xf[28];
#pragma unroll
  for (int c = 0; c < 7; c++) {
    if (c == 0) { WAITV(47); BAR(); STAMP(2); }
    if (c == 1) { WAITV(41); BAR(); }
    if (c == 2) { WAITV(35); BAR(); STAMP(3); }
    if (c == 3) { WAITV(29); BAR(); }
    if (c == 4) { WAITV(23); BAR(); STAMP(4); }
    if (c >= 3 && c < 6) {
#pragma unroll
      for (int i = 0; i < 4; i++) *(LAS v4u*)(L + xs + (c - 3) * 4096 + i * 1024 + lane * 16) = xr[(c - 3) * 4 + i];
    }
#pragma unroll
    for (int s4 = 0; s4 < 4; s4++) {
      const int s = 4 * c + s4;
      xf[s] = lds16(L, xs + (c & 3) * 4096 + r * 128 + (((2 * s4 + hh) ^ xsw) * 16));
      const v4u wk = cat(ldstr(L, L1_W + 28672 + s * 1024 + trb), ldstr(L, L1_W + 28672 + s * 1024 + 256 + trb));
      const v4u wv = cat(ldstr(L, L1_W + 57344 + s * 1024 + trb), ldstr(L, L1_W + 57344 + s * 1024 + 256 + trb));
      ak = mfma32(xf[s], wk, ak);
      av = mfma32(xf[s], wv, av);
      sq = mfma4(xf[s].x, xf[s].y, xf[s].x, xf[s].y, sq);
      sm = mfma4(ONES, ONES, xf[s].x, xf[s].y, sm);
      sq = mfma4(xf[s].z, xf[s].w, xf[s].z, xf[s].w, sq);
      sm = mfma4(ONES, ONES, xf[s].z, xf[s].w, sm);
      if (s4 == w) {
        ck = mfma4(ONES, ONES, wk.x, wk.y, ck); cv = mfma4(ONES, ONES, wv.x, wv.y, cv);
        ck = mfma4(ONES, ONES, wk.z, wk.w, ck); cv = mfma4(ONES, ONES, wv.z, wv.w, cv);
      }
    }
  }
  STAMP(5);
  float ssq = (lane & 2) ? ((lane & 1) ? sq[3] : sq[2]) : ((lane & 1) ? sq[1] : sq[0]);
  ssq = halfsum(ssq);
  const float mean = halfsum(sm[0]) * (1.f / D);
  const float var = fmaxf(ssq * (1.f / D) - mean * mean, 0.f);
  const float rs = __builtin_amdgcn_rsqf(var + 1e-5f), mrs = mean * rs;
  LAS float* stt = (LAS float*)(L + L1_MISC) + w * 64;
  LAS float* csx = (LAS float*)(L + L1_MISC + 1024);
  {
    const float cks = halfsum(ck[0]), cvs = halfsum(cv[0]);
    stt[lane] = hh ? mrs : rs;
    csx[w * 64 + lane] = hh ? cvs : cks;
  }
  WAITV(4);
  BAR();
  STAMP(6);
  const float csk = (csx[r] + csx[64 + r]) + (csx[128 + r] + csx[192 + r]);
  const float csv = (csx[32 + r] + csx[96 + r]) + (csx[160 + r] + csx[224 + r]);
#pragma unroll
  for (int g = 0; g < 4; g++) {
    const f32x4 r4 = *(LAS f32x4*)(stt + 8 * g + 4 * hh), m4 = *(LAS f32x4*)(stt + 32 + 8 * g + 4 * hh);
#pragma unroll
    for (int j = 0; j < 4; j++) {
      ak[4 * g + j] = phi6(fmaf(ak[4 * g + j], r4[j], fmaf(-m4[j], csk, 1e-6f)));
      av[4 * g + j] = fmaf(av[4 * g + j], r4[j], -m4[j] * csv);
    }
  }
  {
    v4u p0 = packfrag<0>(ak), p1 = packfrag<1>(ak), v0 = packfrag<0>(av), v1 = packfrag<1>(av);
    if (r == (odd ? 0 : 28)) { v0 = v4u{ONES, ONES, ONES, ONES}; v1 = v0; }
    f32x16 kv = {};
    kv = mfma32(p0, v0, kv);
    kv = mfma32(p1, v1, kv);
    LAS char* red = L + L1_X;
#pragma unroll
    for (int q = 0; q < 4; q++) *(LAS f32x4*)(red + (q * 4 + w) * 1024 + lane * 16) = f32x4{kv[4 * q], kv[4 * q + 1], kv[4 * q + 2], kv[4 * q + 3]};
    BAR();
    f32x4 t = *(LAS f32x4*)(red + (w * 4 + 0) * 1024 + lane * 16) + *(LAS f32x4*)(red + (w * 4 + 1) * 1024 + lane * 16);
    t += *(LAS f32x4*)(red + (w * 4 + 2) * 1024 + lane * 16) + *(LAS f32x4*)(red + (w * 4 + 3) * 1024 + lane * 16);
    st16(__builtin_bit_cast(v4u, t), rsS, lane * 16, WS_KVP + ((head * 16 + tm) * 4 + w) * 1024);
  }
  STAMP(7);
  f32x16 aq = {};
  f32x4 cq = {};
#pragma unroll
  for (int s = 0; s < 28; s++) {
    const v4u wq = cat(ldstr(L, L1_W + s * 1024 + trb), ldstr(L, L1_W + s * 1024 + 256 + trb));
    aq = mfma32(wq, xf[s], aq);
    cq = mfma4(ONES, ONES, wq.x, wq.y, cq);
    cq = mfma4(ONES, ONES, wq.z, wq.w, cq);
  }
  {
    LAS float* cqx = (LAS float*)(L + L1_MISC + 2048) + w * 64;
    cqx[lane] = halfsum(cq[0]);
#pragma unroll
    for (int g = 0; g < 4; g++) {
      const f32x4 c4 = *(LAS f32x4*)(cqx + 8 * g + 4 * hh);
#pragma unroll
      for (int j = 0; j < 4; j++) aq[4 * g + j] = phi6(fmaf(aq[4 * g + j], rs, fmaf(-mrs, c4[j], 1e-6f)));
    }
    const int tile = tm * 4 + w;
    st16(packfrag<0>(aq), rsS, lane * 16, WS_PQ + (tile * 32 + 2 * head) * 1024);
    st16(packfrag<1>(aq), rsS, lane * 16, WS_PQ + (tile * 32 + 2 * head + 1) * 1024);
  }
  STAMP(8);
  {
    LAS char* stg = L + L1_X + 16384 + w * 4096;
#pragma unroll
    for (int i = 0; i < 4; i++) {
      v4u v = pt[i];
      if (pkind == 2) {
        const int e = 8 * i + (lane >> 3);
        const bool hodd = (prow % 28) != 0 || false;
        const bool pad = (((hu - 784) / 7) & 1) ? (e < 4) : (e >= 28);
        (void)hodd;
        if (pad) v = v4u{0, 0, 0, 0};
      }
      *(LAS v4u*)(stg + i * 1024 + lane * 16) = v;
    }
    const int tb = ((lane >> 5) * 4 + ((lane & 15) >> 2)) * 128 + ((lane >> 4) & 1) * 32 + (lane & 3) * 8;
    f32x4 c0 = {}, c1 = {};
#pragma unroll
    for (int s = 0; s < 2; s++) {
      const v4u f0 = cat(ldstr(stg, s * 2048 + tb), ldstr(stg, s * 2048 + 1024 + tb));
      const v4u f1 = cat(ldstr(stg, s * 2048 + 64 + tb), ldstr(stg, s * 2048 + 1024 + 64 + tb));
      st16(f0, rsS, lane * 16, pdst0 + s * 1024);
      st16(f1, rsS, lane * 16, pdst1 + s * 1024);
      c0 = mfma4(ONES, ONES, f0.x, f0.y, c0); c0 = mfma4(ONES, ONES, f0.z, f0.w, c0);
      c1 = mfma4(ONES, ONES, f1.x, f1.y, c1); c1 = mfma4(ONES, ONES, f1.z, f1.w, c1);
    }
    const float s0 = halfsum(c0[0]), s1 = halfsum(c1[0]);
    if (pkind == 0) __builtin_amdgcn_raw_buffer_store_b32(__builtin_bit_cast(unsigned, hh ? s1 : s0), rsS, lane * 4, WS_C1P + (prow / 32) * (F * 4) + pcol * 2, 0);
  }
  STAMP(9); WAITV(0); STAMP(10); STAMP_OUT(0, 10);
}

__global__ __launch_bounds__(256, 1) void k1b(unsigned char* __restrict__ ws) {
  __shared__ __attribute__((aligned(16))) char smem_[2048];
  LAS char* L = (LAS char*)smem_;
  const int b = blockIdx.x, xcd = b & 7, idx = b >> 3, tm = 2 * xcd + (idx >> 4), head = idx & 15, odd = head & 1;
  const int lane = threadIdx.x & 63, w = __builtin_amdgcn_readfirstlane(threadIdx.x >> 6), hh = lane >> 5;
  const rsrc_t rsS = mkrs(ws, WS_END);
  STAMP_DECL; STAMP(11);
  const int tile = tm * 4 + w;
  v4u p[16];
#pragma unroll
  for (int i = 0; i < 16; i++) p[i] = ld16(rsS, lane * 16, WS_KVP + ((head * 16 + i) * 4 + w) * 1024);
  const v4u q0 = ld16(rsS, lane * 16, WS_PQ + (tile * 32 + 2 * head) * 1024), q1 = ld16(rsS, lane * 16, WS_PQ + (tile * 32 + 2 * head + 1) * 1024);
  __builtin_amdgcn_sched_barrier(0);
  f32x4 t = __builtin_bit_cast(f32x4, p[0]);
#pragma unroll
  for (int i = 1; i < 16; i++) t += __builtin_bit_cast(f32x4, p[i]);
  v2u o2 = {pk2(t[0], t[1]), pk2(t[2], t[3])};
  if (odd ? (w == 0 && hh == 0) : (w == 3 && hh == 1)) o2 = v2u{0, 0};
  *(LAS v2u*)(L + (w >> 1) * 1024 + lane * 16 + (w & 1) * 8) = o2;
  BAR();
  const v4u k0 = lds16(L, lane * 16), k1f = lds16(L, 1024 + lane * 16);
  f32x16 o = {};
  o = mfma32(k0, q0, o);
  o = mfma32(k1f, q1, o);
  const unsigned tu = __builtin_bit_cast(unsigned, odd ? o[0] : o[12]);
  unsigned tu2 = tu;
  asm("" : "+v"(tu2));
  auto sw = __builtin_amdgcn_permlane32_swap(tu, tu2, false, false);
  const float den = __builtin_bit_cast(float, odd ? sw[0] : sw[1]);
  const float inv = __builtin_amdgcn_rcpf(den + 1e-6f);
#pragma unroll
  for (int i = 0; i < 16; i++) o[i] *= inv;
  st16(packfrag<0>(o), rsS, lane * 16, WS_OF + (tile * 32 + 2 * head) * 1024);
  st16(packfrag<1>(o), rsS, lane * 16, WS_OF + (tile * 32 + 2 * head + 1) * 1024);
  STAMP(12); WAITV(0); STAMP(13); STAMP_OUT(11, 13);
}

template <int NKS, int WIN, int WSTR, int ASTR>
DI f32x16 gemm64(LAS char* L, rsrc_t rsS, int lane, int w, int wbase, int abase) {
  v4u fw0[WIN], fw1[WIN], fa0[WIN], fa1[WIN];
  const int vo = lane * 16;
#pragma unroll
  for (int i = 0; i < WIN; i++) {
    fw0[i] = ld16(rsS, vo, wbase + i * 1024); fa0[i] = ld16(rsS, vo, abase + i * 1024);
    fw1[i] = ld16(rsS, vo, wbase + WSTR + i * 1024); fa1[i] = ld16(rsS, vo, abase + ASTR + i * 1024);
  }
  __builtin_amdgcn_sched_barrier(0);
  f32x16 a00 = {}, a01 = {}, a10 = {}, a11 = {};
#pragma unroll
  for (int i = 0; i < NKS; i++) {
    const v4u w0 = fw0[i % WIN], w1 = fw1[i % WIN], x0 = fa0[i % WIN], x1 = fa1[i % WIN];
    a00 = mfma32(w0, x0, a00);
    a10 = mfma32(w1, x0, a10);
    a01 = mfma32(w0, x1, a01);
    a11 = mfma32(w1, x1, a11);
    if (i + WIN < NKS) {
      fw0[i % WIN] = ld16(rsS, vo, wbase + (i + WIN) * 1024); fa0[i % WIN] = ld16(rsS, vo, abase + (i + WIN) * 1024);
      fw1[i % WIN] = ld16(rsS, vo, wbase + WSTR + (i + WIN) * 1024); fa1[i % WIN] = ld16(rsS, vo, abase + ASTR + (i + WIN) * 1024);
    }
    __builtin_amdgcn_sched_barrier(0);
  }
  f32x16 own;
#define PUT(T, A) \
  if (w == T) own = A; \
  else { const int sl = w - (w > T ? 1 : 0); _Pragma("unroll") for (int q = 0; q < 4; q++) \
    *(LAS f32x4*)(L + ((T * 3 + sl) * 4 + q) * 1024 + vo) = f32x4{A[4 * q], A[4 * q + 1], A[4 * q + 2], A[4 * q + 3]}; }
  PUT(0, a00) PUT(1, a10) PUT(2, a01) PUT(3, a11)
#undef PUT
  BAR();
#pragma unroll
  for (int sl = 0; sl < 3; sl++)
#pragma unroll
    for (int q = 0; q < 4; q++) {
      const f32x4 v = *(LAS f32x4*)(L + ((w * 3 + sl) * 4 + q) * 1024 + vo);
#pragma unroll
      for (int j = 0; j < 4; j++) own[4 * q + j] += v[j];
    }
  return own;
}

__global__ __launch_bounds__(256, 1) void k2(const u16* __restrict__ x, const u16* __restrict__ b1, unsigned char* __restrict__ ws) {
  __shared__ __attribute__((aligned(16))) char smem_[49152];
  LAS char* L = (LAS char*)smem_;
  const int b = blockIdx.x, xcd = b & 7, idx = b >> 3;
  const int lane = threadIdx.x & 63, w = __builtin_amdgcn_readfirstlane(threadIdx.x >> 6), hh = lane >> 5, r = lane & 31;
  const rsrc_t rsS = mkrs(ws, WS_END);
  if (idx >= 28) {
    const int f = ((idx - 28) * 8 + xcd) * 256 + threadIdx.x;
    if (f < F) {
      const float* cp = (const float*)(ws + WS_C1P);
      float v[14];
#pragma unroll
      for (int i = 0; i < 14; i++) v[i] = cp[i * F + f];
      float s = 0.f;
#pragma unroll
      for (int i = 0; i < 14; i++) s += v[i];
      const int ft = f >> 5, rr = f & 31, h2 = (rr >> 2) & 1, reg = (rr & 3) + 4 * (rr >> 3);
      float* o = (float*)(ws + WS_C1B) + ft * 64 + h2 * 16 + reg;
      o[0] = s;
      o[32] = bflo(b1[f]);
    }
    return;
  }
  const int g = 4 * xcd + idx / 7, j = idx % 7;
  const int tile = 2 * g + (w >> 1), ct = 2 * j + (w & 1);
  STAMP_DECL; STAMP(14);
  const rsrc_t rsX = mkrs(x, S * D * 2);
  v2u xr[4];
#pragma unroll
  for (int q = 0; q < 4; q++) xr[q] = ld8(rsX, (tile * 32 + r) * 896 + hh * 8, ct * 64 + q * 16);
  f32x16 a = gemm64<8, 8, 32 * 1024, 32 * 1024>(L, rsS, lane, w, WS_WOT + (2 * j * 32 + 8 * w) * 1024, WS_OF + (2 * g * 32 + 8 * w) * 1024);
  STAMP(15);
  unsigned hp[8];
  float s1 = 0.f, s2 = 0.f;
#pragma unroll
  for (int q = 0; q < 4; q++) {
    hp[2 * q] = pk2(a[4 * q] + bflo(xr[q].x), a[4 * q + 1] + bfhi(xr[q].x));
    hp[2 * q + 1] = pk2(a[4 * q + 2] + bflo(xr[q].y), a[4 * q + 3] + bfhi(xr[q].y));
  }
  st16(v4u{hp[0], hp[1], hp[2], hp[3]}, rsS, lane * 16, WS_HF + (tile * 28 + 2 * ct) * 1024);
  st16(v4u{hp[4], hp[5], hp[6], hp[7]}, rsS, lane * 16, WS_HF + (tile * 28 + 2 * ct + 1) * 1024);
#pragma unroll
  for (int q = 0; q < 8; q++) { const float lo = bflo(hp[q]), hi = bfhi(hp[q]); s1 += lo + hi; s2 = fmaf(lo, lo, fmaf(hi, hi, s2)); }
  s1 = halfsum(s1); s2 = halfsum(s2);
  if (hh == 0) st8(v2u{__builtin_bit_cast(unsigned, s1), __builtin_bit_cast(unsigned, s2)}, rsS, r * 8, WS_HST + (tile * 14 + ct) * 256);
  WAITV(0); STAMP(16); STAMP_OUT(14, 16);
}

DI float gelu_f(float y) {
  constexpr float C1 = -2.f * 0.7978845608028654f * LOG2E, C2 = C1 * 0.044715f;
  const float e = __builtin_amdgcn_exp2f(y * fmaf(y * y, C2, C1));
  return y * __builtin_amdgcn_rcpf(1.f + e);
}
constexpr int L3_ST = 114688, L3_BYTES = L3_ST + 1024;
__global__ __launch_bounds__(256, 1) void k3(unsigned char* __restrict__ ws) {
  extern __shared__ __attribute__((aligned(16))) char smem_[];
  LAS char* L = (LAS char*)smem_;
  const int b = blockIdx.x, xcd = b & 7, idx = b >> 3;
  if (idx >= 28) return;
  const int lane = threadIdx.x & 63, w = __builtin_amdgcn_readfirstlane(threadIdx.x >> 6), hh = lane >> 5, r = lane & 31;
  const rsrc_t rsS = mkrs(ws, WS_END);
  const int tm = 2 * xcd + idx / 14, ft = 4 * (idx % 14) + w, vo = lane * 16;
  STAMP_DECL; STAMP(17);
  v4u hr[4][7], wf[28];
  v2u st[7];
  f32x4 cc[4], bb[4];
#pragma unroll
  for (int i = 0; i < 7; i++) hr[0][i] = ld16(rsS, vo, WS_HF + ((tm * 4) * 28 + 7 * w + i) * 1024);
#pragma unroll
  for (int i = 0; i < 7; i++) st[i] = ld8(rsS, lane * 8, WS_HST + ((tm * 4 + w) * 14 + 2 * i) * 256);
#pragma unroll
  for (int i = 0; i < 28; i++) wf[i] = ld16(rsS, vo, WS_W1T + (ft * 28 + i) * 1024);
#pragma unroll
  for (int q = 0; q < 4; q++) {
    cc[q] = __builtin_bit_cast(f32x4, ld16(rsS, hh * 64, WS_C1B + ft * 256 + q * 16));
    bb[q] = __builtin_bit_cast(f32x4, ld16(rsS, hh * 64, WS_C1B + ft * 256 + 128 + q * 16));
  }
#pragma unroll
  for (int i = 0; i < 7; i++) hr[1][i] = ld16(rsS, vo, WS_HF + ((tm * 4 + 1) * 28 + 7 * w + i) * 1024);
  __builtin_amdgcn_sched_barrier(0);
#pragma unroll
  for (int i = 0; i < 7; i++) *(LAS v4u*)(L + (7 * w + i) * 1024 + vo) = hr[0][i];
  {
    float s1 = 0.f, s2 = 0.f;
#pragma unroll
    for (int i = 0; i < 7; i++) { s1 += asf(st[i].x); s2 += asf(st[i].y); }
    s1 = halfsum(s1); s2 = halfsum(s2);
    const float mean = s1 * (1.f / D), var = fmaxf(s2 * (1.f / D) - mean * mean, 0.f);
    const float rs = __builtin_amdgcn_rsqf(var + 1e-5f);
    if (hh == 0) *(LAS f32x2*)(L + L3_ST + w * 256 + r * 8) = f32x2{rs, -mean * rs};
  }
#pragma unroll
  for (int i = 0; i < 7; i++) hr[2][i] = ld16(rsS, vo, WS_HF + ((tm * 4 + 2) * 28 + 7 * w + i) * 1024);
#pragma unroll
  for (int i = 0; i < 7; i++) hr[3][i] = ld16(rsS, vo, WS_HF + ((tm * 4 + 3) * 28 + 7 * w + i) * 1024);
  f32x16 acc[2];
#pragma unroll
  for (int t = 0; t <= 4; t++) {
    if (t < 4) {
      BAR();
      STAMP(18 + t);
      acc[t & 1] = f32x16{};
#pragma unroll
      for (int i = 0; i < 28; i++) acc[t & 1] = mfma32(wf[i], lds16(L, (t * 28 + i) * 1024 + vo), acc[t & 1]);
    }
    if (t > 0) {
      const f32x2 sv = *(LAS f32x2*)(L + L3_ST + (t - 1) * 256 + r * 8);
      f32x16& a = acc[(t - 1) & 1];
#pragma unroll
      for (int q = 0; q < 4; q++)
#pragma unroll
        for (int j = 0; j < 4; j++) a[4 * q + j] = gelu_f(fmaf(a[4 * q + j], sv.x, fmaf(sv.y, cc[q][j], bb[q][j])));
      st16(packfrag<0>(a), rsS, vo, WS_H1F + ((tm * 4 + t - 1) * 112 + 2 * ft) * 1024);
      st16(packfrag<1>(a), rsS, vo, WS_H1F + ((tm * 4 + t - 1) * 112 + 2 * ft + 1) * 1024);
    }
    if (t < 3) {
#pragma unroll
      for (int i = 0; i < 7; i++) *(LAS v4u*)(L + ((t + 1) * 28 + 7 * w + i) * 1024 + vo) = hr[t + 1][i];
    }
    if (t == 3) STAMP(22);
  }
  STAMP(23); WAITV(0); STAMP(24); STAMP_OUT(17, 24);
}

__global__ __launch_bounds__(256, 1) void k4(const u16* __restrict__ b2, u16* __restrict__ out, unsigned char* __restrict__ ws) {
  __shared__ __attribute__((aligned(16))) char smem_[49152];
  LAS char* L = (LAS char*)smem_;
  const int b = blockIdx.x, xcd = b & 7, idx = b >> 3;
  if (idx >= 28) return;
  const int lane = threadIdx.x & 63, w = __builtin_amdgcn_readfirstlane(threadIdx.x >> 6), hh = lane >> 5, r = lane & 31;
  const rsrc_t rsS = mkrs(ws, WS_END), rsB = mkrs(b2, D * 2), rsO = mkrs(out, S * D * 2);
  const int g = 4 * xcd + idx / 7, j = idx % 7;
  const int tile = 2 * g + (w >> 1), ct = 2 * j + (w & 1);
  STAMP_DECL; STAMP(25);
  const v4u h0 = ld16(rsS, lane * 16, WS_HF + (tile * 28 + 2 * ct) * 1024), h1 = ld16(rsS, lane * 16, WS_HF + (tile * 28 + 2 * ct + 1) * 1024);
  v2u br[4];
#pragma unroll
  for (int q = 0; q < 4; q++) br[q] = ld8(rsB, hh * 8, ct * 64 + q * 16);
  f32x16 a = gemm64<28, 10, 112 * 1024, 112 * 1024>(L, rsS, lane, w, WS_W2T + (2 * j * 112 + 28 * w) * 1024, WS_H1F + (2 * g * 112 + 28 * w) * 1024);
  STAMP(26);
  const unsigned hp[8] = {h0.x, h0.y, h0.z, h0.w, h1.x, h1.y, h1.z, h1.w};
#pragma unroll
  for (int q = 0; q < 4; q++) {
    v2u o;
    o.x = pk2(a[4 * q] + bflo(br[q].x) + bflo(hp[2 * q]), a[4 * q + 1] + bfhi(br[q].x) + bfhi(hp[2 * q]));
    o.y = pk2(a[4 * q + 2] + bflo(br[q].y) + bflo(hp[2 * q + 1]), a[4 * q + 3] + bfhi(br[q].y) + bfhi(hp[2 * q + 1]));
    st8(o, rsO, (tile * 32 + r) * 896 + hh * 8, ct * 64 + q * 16);
  }
  WAITV(0); STAMP(27); STAMP_OUT(25, 27);
}

__global__ void k_report(unsigned char* ws) {
  __shared__ unsigned long long sa[256], sb[256];
  const unsigned long long* sp = (const unsigned long long*)(ws + WS_STAMP);
  const int t = threadIdx.x;
  const bool valid = (t >> 3) < 28;
  sa[t] = valid ? sp[t * 32 + 0] : sp[0]; sb[t] = valid ? sp[t * 32 + 0] : sp[0];
  __syncthreads();
  unsigned long long a = sa[0], b = sb[0];
  for (int k = 1; k < 256; k++) { a = min(a, sa[k]); b = min(b, sb[k]); }
  unsigned long long n = (b - a) * 4ull;
  if (n > 20000ull) n = 20000ull;
  unsigned long long t0 = __builtin_amdgcn_s_memrealtime();
  while (__builtin_amdgcn_s_memrealtime() - t0 < n) __builtin_amdgcn_s_sleep(1);
}
extern "C" void kernel_launch(void* const* d_in, const int* in_sizes, int n_in, void* d_out, int out_size, void* d_ws, size_t ws_size,
                              hipStream_t stream) {
  const u16* x = (const u16*)d_in[0];
  const u16* wqkv = (const u16*)d_in[1];
  const u16* wo = (const u16*)d_in[2];
  const u16* w1 = (const u16*)d_in[4];
  const u16* b1 = (const u16*)d_in[5];
  const u16* w2 = (const u16*)d_in[6];
  const u16* b2 = (const u16*)d_in[7];
  unsigned char* ws = (unsigned char*)d_ws;
  (void)hipFuncSetAttribute((const void*)k1, hipFuncAttributeMaxDynamicSharedMemorySize, L1_BYTES);
  (void)hipFuncSetAttribute((const void*)k3, hipFuncAttributeMaxDynamicSharedMemorySize, L3_BYTES);
  k1<<<256, 256, L1_BYTES, stream>>>(x, wqkv, wo, w1, w2, ws);
  k1b<<<256, 256, 0, stream>>>(ws);
  k2<<<256, 256, 0, stream>>>(x, b1, ws);
  k3<<<256, 256, L3_BYTES, stream>>>(ws);
  k4<<<256, 256, 0, stream>>>(b2, (u16*)d_out, ws);
  k_report<<<1, 256, 0, stream>>>((unsigned char*)d_ws);
}
```

```cpp
#include <hip/hip_runtime.h>
#include <hip/hip_bf16.h>
#include <stdint.h>

typedef unsigned short u16;
typedef unsigned v4u __attribute__((ext_vector_type(4)));
typedef unsigned v2u __attribute__((ext_vector_type(2)));
typedef float f32x2 __attribute__((ext_vector_type(2)));
typedef float f32x4 __attribute__((ext_vector_type(4)));
typedef float f32x16 __attribute__((ext_vector_type(16)));
typedef __bf16 bf16x8 __attribute__((ext_vector_type(8)));
typedef __bf16 bf16x4 __attribute__((ext_vector_type(4)));
typedef __bf16 bf16x2 __attribute__((ext_vector_type(2)));
typedef short s16x4 __attribute__((ext_vector_type(4)));
#define LAS __attribute__((address_space(3)))
#define DI __device__ __forceinline__
typedef __amdgpu_buffer_rsrc_t rsrc_t;

constexpr int S = 2048, D = 448, F = 1792;
constexpr int WS_EPOCH = 0;
constexpr int WS_FLAG1 = 4096;
constexpr int WS_FLAG2 = 8192;
constexpr int WS_FLAG3 = 16384;
constexpr int WS_KVP = 65536;
constexpr int WS_OF = WS_KVP + 16 * 16 * 4096;
constexpr int WS_HF = WS_OF + 64 * 32 * 1024;
constexpr int WS_HST = WS_HF + 64 * 28 * 1024;
constexpr int WS_H1F = WS_HST + 64 * 14 * 256;
constexpr int WS_W2T = WS_H1F + 64 * 112 * 1024;
constexpr int WS_END = WS_W2T + 14 * 112 * 1024;
constexpr int OOB = 0x7ff00000;
#define STAMP(i)
#define STAMP_DECL
#define STAMP_OUT(lo, hi)

template <int I> struct IC { static constexpr int v = I; constexpr operator int() const { return I; } };
template <int LO, int HI, class Fn> DI void sfor(Fn&& fn) {
  if constexpr (LO < HI) { fn(IC<LO>{}); sfor<LO + 1, HI>(fn); }
}
#define SFOR(i, lo, hi) sfor<(lo), (hi)>([&](auto i##_) __attribute__((always_inline)) { constexpr int i = decltype(i##_)::v;
#define SEND });

DI f32x16 mfma32(v4u a, v4u b, f32x16 c) {
  return __builtin_amdgcn_mfma_f32_32x32x16_bf16(__builtin_bit_cast(bf16x8, a), __builtin_bit_cast(bf16x8, b), c, 0, 0, 0);
}
DI f32x4 mfma4(unsigned a0, unsigned a1, unsigned b0, unsigned b1, f32x4 c) {
  v2u a = {a0, a1}, b = {b0, b1};
  return __builtin_amdgcn_mfma_f32_4x4x4bf16_1k(__builtin_bit_cast(s16x4, a), __builtin_bit_cast(s16x4, b), c, 0, 0, 0);
}
DI unsigned pk2(float lo, float hi) { bf16x2 v = {(__bf16)lo, (__bf16)hi}; return __builtin_bit_cast(unsigned, v); }
DI float asf(unsigned u) { return __builtin_bit_cast(float, u); }
DI unsigned asu(float f) { return __builtin_bit_cast(unsigned, f); }
DI float bflo(unsigned u) { return asf(u << 16); }
DI float bfhi(unsigned u) { return asf(u & 0xffff0000u); }
DI float halfsum(float a) {
  unsigned u = asu(a);
  auto r = __builtin_amdgcn_permlane32_swap(u, u, false, false);
  const unsigned r0 = r[0], r1 = r[1];
  return asf(r0) + asf(r1);
}
DI rsrc_t mkrs(const void* p, int bytes) { return __builtin_amdgcn_make_buffer_rsrc((void*)p, 0, bytes, 0x00020000); }
constexpr int SC1 = 16;
template <int AUX> DI v4u ld16(rsrc_t rs, int voff, int soff) { return __builtin_amdgcn_raw_buffer_load_b128(rs, voff, soff, AUX); }
template <int AUX> DI v2u ld8(rsrc_t rs, int voff, int soff) { return __builtin_amdgcn_raw_buffer_load_b64(rs, voff, soff, AUX); }
template <int AUX> DI unsigned ld4(rsrc_t rs, int voff, int soff) { return __builtin_amdgcn_raw_buffer_load_b32(rs, voff, soff, AUX); }
template <int AUX> DI void st16(v4u v, rsrc_t rs, int voff, int soff) { __builtin_amdgcn_raw_buffer_store_b128(v, rs, voff + soff, 0, AUX); }
template <int AUX> DI void st8(v2u v, rsrc_t rs, int voff, int soff) { __builtin_amdgcn_raw_buffer_store_b64(v, rs, voff, soff, AUX); }
template <int AUX> DI void st4(unsigned v, rsrc_t rs, int voff, int soff) { __builtin_amdgcn_raw_buffer_store_b32(v, rs, voff, soff, AUX); }
DI void dma16(rsrc_t rs, int voff, int soff, int lds) {
  unsigned keep;
  asm volatile("s_mov_b32 %0, m0\n\ts_mov_b32 m0, %1\n\ts_nop 0\n\tbuffer_load_dwordx4 %2, %3, %4 offen lds\n\ts_mov_b32 m0, %0"
               : "=&s"(keep) : "s"(lds), "v"(voff), "s"(rs), "s"(soff) : "memory");
}
template <int N> DI void waitv() { asm volatile("s_waitcnt vmcnt(%0)" ::"n"(N) : "memory"); }
#define BAR() asm volatile("s_waitcnt lgkmcnt(0)\n\ts_barrier" ::: "memory")
#define SB() __builtin_amdgcn_sched_barrier(0)
#define MEMFENCE() asm volatile("" ::: "memory")
DI v4u lds16(LAS char* p, int off) { return *(LAS v4u*)(p + off); }
DI v2u ldstr(LAS char* p, int off) { return __builtin_bit_cast(v2u, __builtin_amdgcn_ds_read_tr16_b64_v4bf16((LAS bf16x4*)(p + off))); }
DI v4u cat(v2u a, v2u b) { return v4u{a.x, a.y, b.x, b.y}; }
DI v4u ldsfrag_tr(LAS char* p, int off) { return cat(ldstr(p, off), ldstr(p, off + 256)); }
template <int SS> DI v4u packfrag(const f32x16& a) {
  return v4u{pk2(a[8 * SS], a[8 * SS + 1]), pk2(a[8 * SS + 2], a[8 * SS + 3]), pk2(a[8 * SS + 4], a[8 * SS + 5]), pk2(a[8 * SS + 6], a[8 * SS + 7])};
}
constexpr unsigned ONES = 0x3F803F80u, POISON = 0xFFFFFFFFu;
constexpr float LOG2E = 1.4426950408889634f;
constexpr int SPIN_CAP = 1 << 16;
#define ROT ((tm + 2 * head) % 7)
#define TOUCH 1
#define ROT1 (4 * j + (xcd & 3))
#define NW0 8
#define NW1 12
#define NW2 20
#define NW3 6
DI float clamp01(float x) { return __builtin_amdgcn_fmed3f(x, 0.f, 1.f); }
DI float phi6(float tp) { return fmaxf(tp, 1e-6f) + clamp01(__builtin_amdgcn_exp2f(tp * LOG2E)); }
DI int opaque(int v) { asm volatile("" : "+v"(v)); return v; }
DI unsigned umax3(unsigned a, unsigned b, unsigned c) { return max(max(a, b), c); }
DI unsigned vmax(unsigned m, v4u a) { return umax3(umax3(m, a.x, a.y), a.z, a.w); }
DI float gelu_f(float y) {
  constexpr float C1 = -2.f * 0.7978845608028654f * LOG2E, C2 = C1 * 0.044715f;
  const float e = __builtin_amdgcn_exp2f(y * fmaf(y * y, C2, C1));
  return y * __builtin_amdgcn_rcpf(1.f + e);
}

DI void flag_wait(unsigned f, rsrc_t rs, int voff, int base, unsigned tag) {
  if (__builtin_expect(__any(f != tag), 0)) {
    for (int spin = 0; spin < SPIN_CAP; spin++) {
      __builtin_amdgcn_s_sleep(1);
      MEMFENCE();
      if (!__any(ld4<SC1>(rs, voff, base) != tag)) break;
    }
  }
  MEMFENCE();
}

constexpr int L_W = 0, L_X = 86016, L_MISC = L_X + 65536;
constexpr int L_STT = L_MISC, L_CSX = L_MISC + 1024, L_CQX = L_MISC + 2048, L_KVF = L_MISC + 3072, L_ST2 = L_MISC + 5120, L_C1 = L_ST2 + 512;
constexpr int L_BYTES = L_C1 + 2048;
constexpr int L_RED = L_X, L_H = 0, L_HP = L_X + 49152, L_TR = 57344;

DI void transpose_unit(LAS char* stg, int wro, int rdo, v4u lo, v4u hi, v4u& f0, v4u& f1) {
  *(LAS v4u*)(stg + wro) = lo;
  *(LAS v4u*)(stg + 1024 + wro) = hi;
  f0 = cat(ldstr(stg, rdo), ldstr(stg, 1024 + rdo));
  f1 = cat(ldstr(stg, rdo ^ 64), ldstr(stg, 1024 + (rdo ^ 64)));
}

DI f32x16 reduce4(LAS char* L, int w, int vo, const f32x16& a00, const f32x16& a10, const f32x16& a01, const f32x16& a11) {
  f32x16 own;
#define PUT(T, A) \
  if (w == T) own = A; \
  else { const int sl = w - (w > T ? 1 : 0); _Pragma("unroll") for (int q = 0; q < 4; q++) \
    *(LAS f32x4*)(L + L_RED + ((T * 3 + sl) * 4 + q) * 1024 + vo) = f32x4{A[4 * q], A[4 * q + 1], A[4 * q + 2], A[4 * q + 3]}; }
  PUT(0, a00) PUT(1, a10) PUT(2, a01) PUT(3, a11)
#undef PUT
  BAR();
#pragma unroll
  for (int sl = 0; sl < 3; sl++)
#pragma unroll
    for (int q = 0; q < 4; q++) {
      const f32x4 v = *(LAS f32x4*)(L + L_RED + ((w * 3 + sl) * 4 + q) * 1024 + vo);
#pragma unroll
      for (int j = 0; j < 4; j++) own[4 * q + j] += v[j];
    }
  return own;
}

__global__ __launch_bounds__(256, 1) void fused(const u16* __restrict__ x, const u16* __restrict__ wqkv, const u16* __restrict__ wo,
                                                const u16* __restrict__ w1, const u16* __restrict__ b1, const u16* __restrict__ w2,
                                                const u16* __restrict__ b2, u16* __restrict__ out, unsigned char* __restrict__ ws) {
  extern __shared__ __attribute__((aligned(16))) char smem_[];
  LAS char* L = (LAS char*)smem_;
  const int lb = (int)(size_t)L;
  STAMP_DECL; STAMP(0);
  const int b = blockIdx.x, xcd = b & 7, idx = b >> 3, tm = 8 * (xcd >> 2) + (idx >> 2), head = 4 * (xcd & 3) + (idx & 3), odd = head & 1;
  const int lane = threadIdx.x & 63, w = __builtin_amdgcn_readfirstlane(threadIdx.x >> 6), hh = lane >> 5, r = lane & 31, vo = lane * 16;
  const rsrc_t rsX = mkrs(x, S * D * 2), rsW = mkrs(wqkv, D * 3 * D * 2), rsS = mkrs(ws, WS_END);

  const int row0 = tm * 128 + w * 32, atile = tm * 4 + w;
  const int xs = L_X + w * 16384;
  const int voffW = (lane >> 2) * 2688 + (lane & 3) * 16 + ((56 * head) & ~15);
  const int voffX0 = (row0 + (lane >> 3)) * 896 + (((lane & 7) ^ (lane >> 4)) * 16);
  const int voffX1 = (row0 + 8 + (lane >> 3)) * 896 + (((lane & 7) ^ (4 + (lane >> 4))) * 16);
  const int c0 = ROT;
#define CHUNK(i) ((c0 + (i)) >= 7 ? (c0 + (i)) - 7 : (c0 + (i)))
#define DMA_W(m, i) dma16(rsW, voffW, (m) * 896 + (4 * CHUNK(i) + w) * 43008, lb + L_W + (m) * 28672 + (4 * (i) + w) * 1024)
#define DMA_X(i, q) dma16(rsX, ((q) & 1) ? voffX1 : voffX0, ((q) >> 1) * 14336 + CHUNK(i) * 128, lb + xs + ((i) & 3) * 4096 + (q) * 1024)
#define DMA_C(i) do { DMA_W(1, i); DMA_W(2, i); DMA_X(i, 0); DMA_X(i, 1); DMA_X(i, 2); DMA_X(i, 3); } while (0)
  DMA_C(0); DMA_C(1); DMA_C(2);
  SB();
  STAMP(1);
  unsigned epoch;
  asm volatile("s_load_dword %0, %1, 0x0\n\ts_waitcnt lgkmcnt(0)" : "=s"(epoch) : "s"(ws) : "memory");
  const unsigned tag32 = epoch * 1664525u + 1013904223u, tag8 = (epoch + 1u) & 0xFFu;
  const v4u pois = {POISON, POISON, POISON, POISON};
  const bool mlp = idx < 28;
  const int g = 4 * xcd + idx / 7, j = idx % 7;
  const int btile = 2 * g + (w >> 1), ct = 2 * j + (w & 1);
  const int fa = 8 * j + 2 * w;
  const int trb = ((lane >> 5) * 8 + ((lane & 15) >> 2)) * 64 + ((lane >> 4) & 1) * 32 + (lane & 3) * 8;
  const int xrb = xs + r * 128, xsw = (r >> 1) & 7;
  f32x16 ak = {}, av = {};
  f32x4 sq = {}, sm = {}, ck = {}, cv = {};
  v4u xf[28], wk[2][4], wv[2][4], ek, ev;
#define A_READS(c) do { \
    SFOR(s4, 0, 4) \
      xf[4 * (c) + s4] = lds16(L, xrb + ((c) & 3) * 4096 + (((2 * s4 + hh) ^ xsw) * 16)); \
      wk[(c) & 1][s4] = ldsfrag_tr(L, L_W + 28672 + (4 * (c) + s4) * 1024 + trb); \
      wv[(c) & 1][s4] = ldsfrag_tr(L, L_W + 57344 + (4 * (c) + s4) * 1024 + trb); SEND \
    } while (0)
#define A_STEP(c, s4) do { const v4u xx = xf[4 * (c) + (s4)]; \
      ak = mfma32(xx, wk[(c) & 1][s4], ak); av = mfma32(xx, wv[(c) & 1][s4], av); \
      sq = mfma4(xx.x, xx.y, xx.x, xx.y, sq); sm = mfma4(ONES, ONES, xx.x, xx.y, sm); \
      sq = mfma4(xx.z, xx.w, xx.z, xx.w, sq); sm = mfma4(ONES, ONES, xx.z, xx.w, sm); } while (0)
  const int hw = ((idx - 28) * 8 + xcd) * 4 + w;
  const rsrc_t rsO_ = mkrs(mlp ? wo : w2, mlp ? D * D * 2 : D * F * 2), rs1 = mkrs(w1, D * F * 2);
  const int vrow = (lane >> 3) * 896 + (lane & 7) * 16;
  v4u rw[72];
#define PIECE(q) (hw + 128 * (q))
  int rbase[4];
#pragma unroll
  for (int q = 0; q < 4; q++) {
    const int pc = min(PIECE(q), 391), kc = (pc * 9363) >> 16;
    rbase[q] = mlp ? (28 * (4 * w + q) - 4 * (q & 1)) * 896 + j * 128 : 32 * kc * 896 + (pc - 7 * kc) * 128;
  }
  const int vlo = (mlp && lane < 32) ? OOB : vrow, vhi = (mlp && lane >= 32) ? OOB : vrow;
  const int v1 = mlp ? (lane >> 3) * 3584 + (lane & 7) * 16 + fa * 64 : OOB;
  const int r1 = ROT1;
#define W1_KS(k) ((k) + r1 >= 28 ? (k) + r1 - 28 : (k) + r1)
#define LD_RW(i) do { \
    if constexpr ((i) < 16) rw[i] = ld16<0>(rsO_, ((i) & 7) == 4 ? vlo : (((i) & 7) == 3 ? vhi : vrow), rbase[(i) >> 2] + ((i) & 3) * 8 * 896); \
    else rw[i] = ld16<0>(rs1, v1, (2 * W1_KS(((i) - 16) >> 1) + ((i) & 1)) * 8 * 3584); } while (0)
  unsigned tch = 0;
  waitv<12>(); BAR();
  STAMP(2);
  A_READS(0);
  SB();
  SFOR(c, 0, 7)
    ek = ldsfrag_tr(L, L_W + 28672 + (4 * c) * 1024 + w * 1024 + trb);
    ev = ldsfrag_tr(L, L_W + 57344 + (4 * c) * 1024 + w * 1024 + trb);
    A_STEP(c, 0);
    if (c < 4) { DMA_W(1, c + 3); DMA_W(2, c + 3); } else if (c < 6) DMA_W(0, 3 * (c - 4));
    if constexpr (c == 6) { SFOR(i, 0, NW0 / 3) LD_RW(i); SEND }
    SB();
    A_STEP(c, 1);
    if (c < 4) { DMA_X(c + 3, 0); DMA_X(c + 3, 1); } else if (c < 6) DMA_W(0, 3 * (c - 4) + 1);
    if constexpr (c == 6) { SFOR(i, NW0 / 3, 2 * NW0 / 3) LD_RW(i); SEND }
    SB();
    A_STEP(c, 2);
    if (c < 4) { DMA_X(c + 3, 2); DMA_X(c + 3, 3); } else if (c < 6) DMA_W(0, 3 * (c - 4) + 2);
    if constexpr (c == 6) { SFOR(i, 2 * NW0 / 3, NW0) LD_RW(i); SEND }
    if (TOUCH && c == 2) {
      const int t = (b * 4 + w) * 32 + r;
      const u16* tp = t < 3136 ? wo + t * 64 : (t < 15680 ? w1 + (t - 3136) * 64 : w2 + (min(t, 28223) - 15680) * 64);
      asm volatile("global_load_dword %0, %1, off" : "=v"(tch) : "v"(tp) : "memory");
    }
    ck = mfma4(ONES, ONES, ek.x, ek.y, ck); cv = mfma4(ONES, ONES, ev.x, ev.y, cv);
    ck = mfma4(ONES, ONES, ek.z, ek.w, ck); cv = mfma4(ONES, ONES, ev.z, ev.w, cv);
    SB();
    if constexpr (c < 6) {
      if (c < 2) waitv<12>(); else if (c < 4) waitv<12 + TOUCH>(); else if (c == 4) waitv<9 + TOUCH>(); else waitv<6>();
      BAR();
      A_READS(c + 1);
      SB();
    }
    A_STEP(c, 3);
    if (c == 5) DMA_W(0, 6);
    SB();
    STAMP(22 + c);
  SEND
  STAMP(4);
  float ssq = (lane & 2) ? ((lane & 1) ? sq[3] : sq[2]) : ((lane & 1) ? sq[1] : sq[0]);
  ssq = halfsum(ssq);
  const float mean = halfsum(sm[0]) * (1.f / D);
  const float var = fmaxf(ssq * (1.f / D) - mean * mean, 0.f);
  const float rs = __builtin_amdgcn_rsqf(var + 1e-5f), mrs = mean * rs;
  LAS float* stt = (LAS float*)(L + L_STT) + w * 64;
  LAS float* csx = (LAS float*)(L + L_CSX);
  {
    const float cks = halfsum(ck[0]), cvs = halfsum(cv[0]);
    stt[lane] = hh ? mrs : rs;
    csx[w * 64 + lane] = hh ? cvs : cks;
  }
  BAR();
  {
    const float csk = (csx[r] + csx[64 + r]) + (csx[128 + r] + csx[192 + r]);
    const float csv = (csx[32 + r] + csx[96 + r]) + (csx[160 + r] + csx[224 + r]);
#pragma unroll
    for (int q = 0; q < 4; q++) {
      const f32x4 r4 = *(LAS f32x4*)(stt + 8 * q + 4 * hh), m4 = *(LAS f32x4*)(stt + 32 + 8 * q + 4 * hh);
#pragma unroll
      for (int i = 0; i < 4; i++) {
        ak[4 * q + i] = phi6(fmaf(ak[4 * q + i], r4[i], fmaf(-m4[i], csk, 1e-6f)));
        av[4 * q + i] = fmaf(av[4 * q + i], r4[i], -m4[i] * csv);
      }
    }
    v4u p0 = packfrag<0>(ak), p1 = packfrag<1>(ak), v0 = packfrag<0>(av), v1 = packfrag<1>(av);
    if (r == (odd ? 0 : 28)) { v0 = v4u{ONES, ONES, ONES, ONES}; v1 = v0; }
    f32x16 kv = {};
    kv = mfma32(p0, v0, kv);
    kv = mfma32(p1, v1, kv);
    LAS char* red = L + L_X;
#pragma unroll
    for (int q = 0; q < 4; q++) *(LAS f32x4*)(red + (q * 4 + w) * 1024 + vo) = f32x4{kv[4 * q], kv[4 * q + 1], kv[4 * q + 2], kv[4 * q + 3]};
    waitv<NW0>();
    asm volatile("" :: "v"(tch));
    BAR();
    f32x4 t = *(LAS f32x4*)(red + (w * 4 + 0) * 1024 + vo) + *(LAS f32x4*)(red + (w * 4 + 1) * 1024 + vo);
    t += *(LAS f32x4*)(red + (w * 4 + 2) * 1024 + vo) + *(LAS f32x4*)(red + (w * 4 + 3) * 1024 + vo);
    v4u tu;
#pragma unroll
    for (int i = 0; i < 4; i++) tu[i] = (asu(t[i]) & ~0xFFu) | tag8;
    st16<SC1>(tu, rsS, vo, WS_KVP + ((head * 16 + tm) * 4 + w) * 1024);
    SB();
  }
  STAMP(5);
  LAS char* stg = L + L_TR + w * 6144;
  const int wro = (lane ^ ((lane >> 2) & 4)) * 16;
  const int rdo = ((lane >> 5) * 4 + ((lane & 15) >> 2)) * 128 + (((lane >> 3) & 1) * 64) + ((lane >> 4) & 1) * 32 + (lane & 3) * 8;
  v4u wo0[8], wo1[8];
  v4u wA[28], wB[28];
  f32x4 ca = {}, cb = {};
#define TR(n) do { \
    if constexpr ((n) < 8) transpose_unit(stg + ((n) % 3) * 2048, wro, rdo, rw[2 * (n)], rw[2 * (n) + 1], wo0[(n) & 7], wo1[(n) & 7]); \
    else { constexpr int k_ = (n) - 8; \
      transpose_unit(stg + ((n) % 3) * 2048, wro, rdo, rw[2 * (n)], rw[2 * (n) + 1], wA[k_], wB[k_]); \
      ca = mfma4(ONES, ONES, wA[k_].x, wA[k_].y, ca); cb = mfma4(ONES, ONES, wB[k_].x, wB[k_].y, cb); \
      ca = mfma4(ONES, ONES, wA[k_].z, wA[k_].w, ca); cb = mfma4(ONES, ONES, wB[k_].z, wB[k_].w, cb); } } while (0)
  constexpr int E1 = 16 + NW1, E2 = E1 + NW2, E3 = E2 + 8 < 72 ? E2 + 8 : 72;
  v4u pq0, pq1;
  {
    const int vow = mlp ? vo : OOB;
    st16<SC1>(pois, rsS, vo, WS_OF + (atile * 32 + 2 * head) * 1024);
    st16<SC1>(pois, rsS, vo, WS_OF + (atile * 32 + 2 * head + 1) * 1024);
    st16<SC1>(pois, rsS, vow, WS_HF + (btile * 28 + 2 * ct) * 1024);
    st16<SC1>(pois, rsS, vow, WS_HF + (btile * 28 + 2 * ct + 1) * 1024);
    st4<SC1>(POISON, rsS, mlp ? lane * 4 : OOB, WS_HST + (btile * 14 + ct) * 256);
#pragma unroll
    for (int k = 0; k < 4; k++) st16<SC1>(pois, rsS, vow, WS_H1F + ((2 * g + (k >> 1)) * 112 + 2 * fa + 2 + (k & 1)) * 1024);
    SB();
    f32x16 aq = {};
    f32x4 cq = {}, cq2 = {};
    v4u wq[4];
    SFOR(s, 0, 4) wq[s] = ldsfrag_tr(L, L_W + s * 1024 + trb); SEND
    SFOR(s, 0, 28)
      const v4u wv_ = wq[s & 3];
      aq = mfma32(wv_, xf[s], aq);
      cq = mfma4(ONES, ONES, wv_.x, wv_.y, cq);
      cq2 = mfma4(ONES, ONES, wv_.z, wv_.w, cq2);
      if constexpr (s + 4 < 28) wq[s & 3] = ldsfrag_tr(L, L_W + (s + 4) * 1024 + trb);
      if constexpr (NW0 + s < E1) LD_RW(NW0 + s);
      if constexpr (s >= 12 && (s & 3) == 0 && (s - 12) / 4 < NW0 / 2) TR((s - 12) / 4);
      SB();
    SEND
    LAS float* cqx = (LAS float*)(L + L_CQX) + w * 64;
    cqx[lane] = halfsum(cq[0] + cq2[0]);
#pragma unroll
    for (int q = 0; q < 4; q++) {
      const f32x4 c4 = *(LAS f32x4*)(cqx + 8 * q + 4 * hh);
#pragma unroll
      for (int i = 0; i < 4; i++) aq[4 * q + i] = phi6(fmaf(aq[4 * q + i], rs, fmaf(-mrs, c4[i], 1e-6f)));
    }
    pq0 = packfrag<0>(aq); pq1 = packfrag<1>(aq);
  }
  SB();
  waitv<E1 - NW0>();
  st4<SC1>(tag32, rsS, lane == 0 ? WS_FLAG2 + (atile >> 1) * 256 + ((atile & 1) * 16 + head) * 4 : (lane == 1 && mlp ? WS_FLAG2 + g * 256 + (32 + j * 4 + w) * 4 : OOB), 0);
  STAMP(6);
  {
    f32x4 t;
    v4u p[16];
#define KV_LOADS() _Pragma("unroll") for (int i = 0; i < 16; i++) p[i] = ld16<SC1>(rsS, vo, WS_KVP + ((head * 16 + i) * 4 + w) * 1024)
#define KV_CHECK() unsigned bad = 0; t = f32x4{0.f, 0.f, 0.f, 0.f}; \
      _Pragma("unroll") for (int i = 0; i < 16; i++) { \
        bad |= (p[i].x ^ tag8) | (p[i].y ^ tag8) | (p[i].z ^ tag8) | (p[i].w ^ tag8); \
        t += __builtin_bit_cast(f32x4, p[i]); } \
      const bool again = __any((bad & 0xFFu) != 0)
    KV_LOADS();
    SB();
    SFOR(n, (NW0 / 2 < 4 ? NW0 / 2 : 4), E1 / 2) TR(n); SEND
    SB();
    {
      KV_CHECK();
      if (again) {
        for (int spin = 0; spin < SPIN_CAP; spin++) {
          MEMFENCE();
          KV_LOADS();
          SB();
          KV_CHECK();
          if (!again) break;
          __builtin_amdgcn_s_sleep(2);
        }
      }
    }
    STAMP(7);
    v2u o2 = {pk2(t[0], t[1]), pk2(t[2], t[3])};
    if (odd ? (w == 0 && hh == 0) : (w == 3 && hh == 1)) o2 = v2u{0, 0};
    *(LAS v2u*)(L + L_KVF + (w >> 1) * 1024 + vo + (w & 1) * 8) = o2;
    BAR();
    const v4u k0 = lds16(L, L_KVF + vo), k1 = lds16(L, L_KVF + 1024 + vo);
    f32x16 o = {};
    o = mfma32(k0, pq0, o);
    o = mfma32(k1, pq1, o);
    const unsigned tu = asu(odd ? o[0] : o[12]);
    auto sw = __builtin_amdgcn_permlane32_swap(tu, tu, false, false);
    const unsigned s0 = sw[0], s1 = sw[1];
    const float inv = __builtin_amdgcn_rcpf(asf(odd ? s0 : s1) + 1e-6f);
#pragma unroll
    for (int i = 0; i < 16; i++) o[i] *= inv;
    st16<SC1>(packfrag<0>(o), rsS, vo, WS_OF + (atile * 32 + 2 * head) * 1024);
    st16<SC1>(packfrag<1>(o), rsS, vo, WS_OF + (atile * 32 + 2 * head + 1) * 1024);
    SB();
  }
  STAMP(8);
  if (!mlp) {
    SFOR(u, 0, 8)
      const int pc = PIECE(u >> 1), kc = (pc * 9363) >> 16, cb_ = pc - 7 * kc;
      const int dst = pc < 392 ? WS_W2T + (2 * cb_ * 112 + 2 * kc + (u & 1)) * 1024 : OOB;
      st16<SC1>(wo0[u], rsS, vo, dst);
      st16<SC1>(wo1[u], rsS, vo, dst + 112 * 1024);
    SEND
    waitv<0>();
    st4<SC1>(tag32, rsS, lane == 0 ? WS_FLAG1 + hw * 4 : OOB, 0);
    STAMP_OUT(0, 31);
    return;
  }
  unsigned f3;
  v4u u0[28], u1[28];
#define D_KS(i) (28 * w + ((i) < 14 ? 4 * ((i) >> 1) + ((i) & 1) : 4 * (((i) - 14) >> 1) + 2 + ((i) & 1)))
#define LD_U(q) do { if constexpr ((q) & 1) u1[(q) >> 1] = ld16<SC1>(rsS, vo, WS_W2T + ((2 * j + 1) * 112 + D_KS((q) >> 1)) * 1024); \
                     else u0[(q) >> 1] = ld16<SC1>(rsS, vo, WS_W2T + ((2 * j) * 112 + D_KS((q) >> 1)) * 1024); } while (0)
  {
    const int fo = (lane < 60 ? lane : 0) * 4;
    const unsigned f2 = ld4<SC1>(rsS, fo, WS_FLAG2 + g * 256);
    v2u xr[4];
#pragma unroll
    for (int q = 0; q < 4; q++) xr[q] = ld8<0>(rsX, (btile * 32 + r) * 896 + hh * 8, ct * 64 + q * 16);
    SFOR(i, E1, E2) LD_RW(i); SEND
    SB();
    flag_wait(f2, rsS, fo, WS_FLAG2 + g * 256, tag32);
    STAMP(9);
    v4u o0[8], o1[8];
#define O_LOADS() _Pragma("unroll") for (int i = 0; i < 8; i++) { \
        o0[i] = ld16<SC1>(rsS, vo, WS_OF + ((2 * g) * 32 + 8 * w + i) * 1024); \
        o1[i] = ld16<SC1>(rsS, vo, WS_OF + ((2 * g + 1) * 32 + 8 * w + i) * 1024); }
#define O_CHECK() unsigned m = 0; _Pragma("unroll") for (int i = 0; i < 8; i++) m = vmax(vmax(m, o0[i]), o1[i]); const bool again = __any(m == POISON)
    O_LOADS();
    SB();
    SFOR(n, E1 / 2, E2 / 2) TR(n); SEND
    SB();
    {
      O_CHECK();
      if (again) {
        for (int spin = 0; spin < SPIN_CAP; spin++) {
          MEMFENCE();
          O_LOADS();
          SB();
          O_CHECK();
          if (!again) break;
          __builtin_amdgcn_s_sleep(2);
        }
      }
    }
    STAMP(10);
    if (b == 0 && threadIdx.x == 0) st4<SC1>(tag32, rsS, 0, WS_EPOCH);
    f32x16 a00 = {}, a10 = {}, a01 = {}, a11 = {};
    SFOR(i, 0, 8)
      a00 = mfma32(wo0[i], o0[i], a00); a10 = mfma32(wo1[i], o0[i], a10);
      a01 = mfma32(wo0[i], o1[i], a01); a11 = mfma32(wo1[i], o1[i], a11);
      if constexpr (E2 + i < E3) LD_RW(E2 + i);
      SB();
    SEND
    const f32x16 a = reduce4(L, w, opaque(vo), a00, a10, a01, a11);
    unsigned hp[8];
#pragma unroll
    for (int q = 0; q < 4; q++) {
      hp[2 * q] = pk2(a[4 * q] + bflo(xr[q].x), a[4 * q + 1] + bfhi(xr[q].x));
      hp[2 * q + 1] = pk2(a[4 * q + 2] + bflo(xr[q].y), a[4 * q + 3] + bfhi(xr[q].y));
    }
    st16<SC1>(v4u{hp[0], hp[1], hp[2], hp[3]}, rsS, vo, WS_HF + (btile * 28 + 2 * ct) * 1024);
    st16<SC1>(v4u{hp[4], hp[5], hp[6], hp[7]}, rsS, vo, WS_HF + (btile * 28 + 2 * ct + 1) * 1024);
    float s1 = 0.f, s2 = 0.f;
#pragma unroll
    for (int q = 0; q < 8; q++) { const float lo = bflo(hp[q]), hi = bfhi(hp[q]); s1 += lo + hi; s2 = fmaf(lo, lo, fmaf(hi, hi, s2)); }
    s1 = halfsum(s1); s2 = halfsum(s2);
    if (hh == 0) st8<SC1>(v2u{asu(s1), asu(s2)}, rsS, r * 8, WS_HST + (btile * 14 + ct) * 256);
    *(LAS v4u*)(L + L_HP + w * 2048 + vo) = v4u{hp[0], hp[1], hp[2], hp[3]};
    *(LAS v4u*)(L + L_HP + w * 2048 + 1024 + vo) = v4u{hp[4], hp[5], hp[6], hp[7]};
    SB();
  }
  STAMP(11);
  {
    const int f1o = (((14 * w + (lane < 14 ? lane : 0)) * 7 + j) & 127) * 4;
    const unsigned f1 = ld4<SC1>(rsS, f1o, WS_FLAG1);
    const float bv = bflo(b1[fa * 32 + lane]);
    SFOR(i, E3, 72) LD_RW(i); SEND
    SB();
    flag_wait(f1, rsS, f1o, WS_FLAG1, tag32);
    SFOR(q, 0, NW3) LD_U(q); SEND
    LAS float* c1x = (LAS float*)(L + L_C1) + w * 128;
    {
      v4u hr[14]; v2u st[7];
#define H_LOADS() _Pragma("unroll") for (int i = 0; i < 14; i++) hr[i] = ld16<SC1>(rsS, vo, WS_HF + ((2 * g) * 28 + 14 * w + i) * 1024); \
        _Pragma("unroll") for (int i = 0; i < 7; i++) st[i] = ld8<SC1>(rsS, lane * 8, WS_HST + ((2 * g + (w & 1)) * 14 + 2 * i) * 256)
#define H_CHECK() unsigned m = 0; _Pragma("unroll") for (int i = 0; i < 14; i++) m = vmax(m, hr[i]); \
        _Pragma("unroll") for (int i = 0; i < 7; i++) m = umax3(m, st[i].x, st[i].y); const bool again = __any(m == POISON)
      H_LOADS();
      SB();
      SFOR(n, E2 / 2, 36) TR(n); SEND
      {
        const float sa = halfsum(ca[0]), sb = halfsum(cb[0]);
        c1x[lane] = hh ? sb : sa; c1x[64 + lane] = bv;
      }
      SB();
      {
        H_CHECK();
        if (again) {
          for (int spin = 0; spin < SPIN_CAP; spin++) {
            MEMFENCE();
            H_LOADS();
            SB();
            H_CHECK();
            if (!again) break;
            __builtin_amdgcn_s_sleep(2);
          }
        }
      }
      STAMP(12);
#pragma unroll
      for (int i = 0; i < 14; i++) {
        const int ks = 14 * (w & 1) + i - r1;
        *(LAS v4u*)(L + L_H + ((w >> 1) * 28 + (ks < 0 ? ks + 28 : ks)) * 1024 + vo) = hr[i];
      }
      float s1 = 0.f, s2 = 0.f;
#pragma unroll
      for (int i = 0; i < 7; i++) { s1 += asf(st[i].x); s2 += asf(st[i].y); }
      s1 = halfsum(s1); s2 = halfsum(s2);
      const float mean2 = s1 * (1.f / D), var2 = fmaxf(s2 * (1.f / D) - mean2 * mean2, 0.f);
      const float rs2 = __builtin_amdgcn_rsqf(var2 + 1e-5f);
      if (w < 2 && hh == 0) *(LAS f32x2*)(L + L_ST2 + w * 256 + r * 8) = f32x2{rs2, -mean2 * rs2};
    }
    BAR();
    STAMP(13);
    const f32x2 sv0 = *(LAS f32x2*)(L + L_ST2 + r * 8), sv1 = *(LAS f32x2*)(L + L_ST2 + 256 + r * 8);
    constexpr int LA = 6;
    v4u hf[LA];
    SFOR(i, 0, LA) hf[i] = lds16(L, L_H + i * 1024 + vo); SEND
    f32x16 acc[2];
    f32x4 cc[2], bb[2];
    unsigned pkd[8];
    float ya[2], eb[2];
    SFOR(T, 0, 5)
      constexpr int P = T - 1;
      if constexpr (T < 4) acc[T & 1] = f32x16{};
      SFOR(i, 0, 28)
        if constexpr (T < 4) {
          constexpr int n = T * 28 + i, nn = n + LA;
          acc[T & 1] = mfma32(T < 2 ? wA[i] : wB[i], hf[n % LA], acc[T & 1]);
          if constexpr (nn < 112) hf[n % LA] = lds16(L, L_H + (((nn / 28) & 1) * 28 + nn % 28) * 1024 + vo);
          if constexpr (n < 56) { if constexpr ((n & 1) == 0 && NW3 + n / 2 < 28) LD_U(NW3 + n / 2); }
          else if constexpr ((n & 1) == 0) LD_U(28 + ((n - 56) >> 1));
        }
        if constexpr (i % 7 == 3) {
          constexpr int nq = (T * 4 + i / 7 + 1), nP = nq / 4 - 1;
          if constexpr (nP >= 0 && nP < 4) {
            cc[nq & 1] = *(LAS f32x4*)(c1x + (nP >> 1) * 32 + 8 * (nq & 3) + 4 * hh);
            bb[nq & 1] = *(LAS f32x4*)(c1x + 64 + (nP >> 1) * 32 + 8 * (nq & 3) + 4 * hh);
          }
        }
        if constexpr (T > 0) {
          const f32x2 sv = (P & 1) ? sv1 : sv0;
          constexpr int m7 = i % 7, pr = 2 * (i / 7) + (m7 >= 3 ? 1 : 0), stage = m7 >= 3 ? m7 - 3 : m7;
          constexpr float C1 = -2.f * 0.7978845608028654f * LOG2E, C2 = C1 * 0.044715f;
          if constexpr (m7 < 6) {
            SFOR(e, 0, 2)
              constexpr int reg = 2 * pr + e;
              if constexpr (stage == 0) {
                ya[e] = fmaf(acc[P & 1][reg], sv.x, fmaf(sv.y, cc[(reg >> 2) & 1][reg & 3], bb[(reg >> 2) & 1][reg & 3]));
                eb[e] = fmaf(ya[e] * ya[e], C2, C1);
              } else if constexpr (stage == 1) {
                eb[e] = 1.f + __builtin_amdgcn_exp2f(ya[e] * eb[e]);
              } else {
                ya[e] = ya[e] * __builtin_amdgcn_rcpf(eb[e]);
              }
            SEND
            if constexpr (stage == 2) pkd[pr] = pk2(ya[0], ya[1]);
            else if constexpr (T < 4) asm volatile("" : "+v"(ya[0]), "+v"(ya[1]), "+v"(eb[0]), "+v"(eb[1]));
          }
          if constexpr (i == 13) st16<SC1>(v4u{pkd[0], pkd[1], pkd[2], pkd[3]}, rsS, vo, WS_H1F + ((2 * g + (P & 1)) * 112 + 2 * (fa + (P >> 1))) * 1024);
          if constexpr (i == 27) st16<SC1>(v4u{pkd[4], pkd[5], pkd[6], pkd[7]}, rsS, vo, WS_H1F + ((2 * g + (P & 1)) * 112 + 2 * (fa + (P >> 1)) + 1) * 1024);
        }
        if constexpr (T == 3 && i == 12) {
          waitv<7>();
          st4<SC1>(tag32, rsS, lane == 0 ? WS_FLAG3 + (g * 32 + j * 4 + w) * 4 : OOB, 0);
        }
        if constexpr (T == 4 && i == 0) f3 = ld4<SC1>(rsS, (r < 28 ? r : 0) * 4, WS_FLAG3 + g * 128);
        if constexpr (T < 4) SB();
      SEND
      if constexpr (T == 1) STAMP(14);
      if constexpr (T == 3) STAMP(15);
    SEND
    SB();
  }
  STAMP(16);
  {
    const rsrc_t rsB = mkrs(b2, D * 2), rsO = mkrs(out, S * D * 2);
    v2u br[4];
#pragma unroll
    for (int q = 0; q < 4; q++) br[q] = ld8<0>(rsB, hh * 8, ct * 64 + q * 16);
    flag_wait(f3, rsS, (r < 28 ? r : 0) * 4, WS_FLAG3 + g * 128, tag32);
    constexpr int WIN = 12;
    v4u x0[WIN], x1[WIN];
    const int hb0 = WS_H1F + (2 * g) * 112 * 1024, hb1 = hb0 + 112 * 1024;
    SFOR(i, 0, WIN) x0[i] = ld16<SC1>(rsS, vo, hb0 + D_KS(i) * 1024); x1[i] = ld16<SC1>(rsS, vo, hb1 + D_KS(i) * 1024); SEND
    SB();
    f32x16 a00 = {}, a10 = {}, a01 = {}, a11 = {};
    SFOR(i, 0, 28)
      v4u y0 = x0[i % WIN], y1 = x1[i % WIN];
      if (i >= 14 && __builtin_expect(__any(vmax(vmax(0u, y0), y1) == POISON), 0)) {
        for (int spin = 0; spin < SPIN_CAP; spin++) {
          __builtin_amdgcn_s_sleep(1);
          MEMFENCE();
          y0 = ld16<SC1>(rsS, vo, hb0 + D_KS(i) * 1024); y1 = ld16<SC1>(rsS, vo, hb1 + D_KS(i) * 1024);
          if (!__any(vmax(vmax(0u, y0), y1) == POISON)) break;
        }
      }
      a00 = mfma32(u0[i], y0, a00); a10 = mfma32(u1[i], y0, a10);
      a01 = mfma32(u0[i], y1, a01); a11 = mfma32(u1[i], y1, a11);
      if constexpr (i + WIN < 28) { x0[i % WIN] = ld16<SC1>(rsS, vo, hb0 + D_KS(i + WIN) * 1024); x1[i % WIN] = ld16<SC1>(rsS, vo, hb1 + D_KS(i + WIN) * 1024); }
      SB();
      if constexpr (i == 0) STAMP(17);
      if constexpr (i == 13) STAMP(18);
    SEND
    STAMP(19);
    const int vd = opaque(vo);
    const f32x16 a = reduce4(L, w, vd, a00, a10, a01, a11);
    const v4u hq0 = lds16(L, L_HP + w * 2048 + vd), hq1 = lds16(L, L_HP + w * 2048 + 1024 + vd);
    const unsigned hp[8] = {hq0.x, hq0.y, hq0.z, hq0.w, hq1.x, hq1.y, hq1.z, hq1.w};
#pragma unroll
    for (int q = 0; q < 4; q++) {
      v2u o;
      o.x = pk2(a[4 * q] + bflo(br[q].x) + bflo(hp[2 * q]), a[4 * q + 1] + bfhi(br[q].x) + bfhi(hp[2 * q]));
      o.y = pk2(a[4 * q + 2] + bflo(br[q].y) + bflo(hp[2 * q + 1]), a[4 * q + 3] + bfhi(br[q].y) + bfhi(hp[2 * q + 1]));
      st8<0>(o, rsO, (btile * 32 + ((vd >> 4) & 31)) * 896 + (vd >> 9) * 8, ct * 64 + q * 16);
    }
  }
}

extern "C" void kernel_launch(void* const* d_in, const int* in_sizes, int n_in, void* d_out, int out_size, void* d_ws, size_t ws_size,
                              hipStream_t stream) {
  (void)hipFuncSetAttribute((const void*)fused, hipFuncAttributeMaxDynamicSharedMemorySize, L_BYTES);
  fused<<<256, 256, L_BYTES, stream>>>((const u16*)d_in[0], (const u16*)d_in[1], (const u16*)d_in[2], (const u16*)d_in[4], (const u16*)d_in[5],
                                       (const u16*)d_in[6], (const u16*)d_in[7], (u16*)d_out, (unsigned char*)d_ws);
}
```

```cpp
#define STAMPS
#include <hip/hip_runtime.h>
#include <hip/hip_bf16.h>
#include <stdint.h>

typedef unsigned short u16;
typedef unsigned v4u __attribute__((ext_vector_type(4)));
typedef unsigned v2u __attribute__((ext_vector_type(2)));
typedef float f32x2 __attribute__((ext_vector_type(2)));
typedef float f32x4 __attribute__((ext_vector_type(4)));
typedef float f32x16 __attribute__((ext_vector_type(16)));
typedef __bf16 bf16x8 __attribute__((ext_vector_type(8)));
typedef __bf16 bf16x4 __attribute__((ext_vector_type(4)));
typedef __bf16 bf16x2 __attribute__((ext_vector_type(2)));
typedef short s16x4 __attribute__((ext_vector_type(4)));
#define LAS __attribute__((address_space(3)))
#define DI __device__ __forceinline__
typedef __amdgpu_buffer_rsrc_t rsrc_t;

constexpr int S = 2048, D = 448, F = 1792;
constexpr int WS_EPOCH = 0;
constexpr int WS_FLAG1 = 4096;
constexpr int WS_FLAG2 = 8192;
constexpr int WS_FLAG3 = 16384;
constexpr int WS_KVP = 65536;
constexpr int WS_OF = WS_KVP + 16 * 16 * 4096;
constexpr int WS_HF = WS_OF + 64 * 32 * 1024;
constexpr int WS_HST = WS_HF + 64 * 28 * 1024;
constexpr int WS_H1F = WS_HST + 64 * 14 * 256;
constexpr int WS_W2T = WS_H1F + 64 * 112 * 1024;
constexpr int WS_END = WS_W2T + 14 * 112 * 1024;
constexpr int OOB = 0x7ff00000;
constexpr int WS_STAMP = WS_END + 4096;
constexpr int NSTAMP = 32;
#define STAMP(i) do { __builtin_amdgcn_sched_barrier(0); asm volatile("s_memrealtime %0\n\ts_waitcnt lgkmcnt(0)" : "=s"(ts_[i]) :: "memory"); __builtin_amdgcn_sched_barrier(0); } while (0)
#define STAMP_DECL unsigned long long ts_[32] = {}
#define STAMP_OUT(lo, hi) do { if (threadIdx.x == 0) { _Pragma("unroll") for (int i_ = lo; i_ <= hi; i_++) ((unsigned long long*)(ws + WS_STAMP))[blockIdx.x * 32 + i_] = ts_[i_]; } } while (0)

template <int I> struct IC { static constexpr int v = I; constexpr operator int() const { return I; } };
template <int LO, int HI, class Fn> DI void sfor(Fn&& fn) {
  if constexpr (LO < HI) { fn(IC<LO>{}); sfor<LO + 1, HI>(fn); }
}
#define SFOR(i, lo, hi) sfor<(lo), (hi)>([&](auto i##_) __attribute__((always_inline)) { constexpr int i = decltype(i##_)::v;
#define SEND });

DI f32x16 mfma32(v4u a, v4u b, f32x16 c) {
  return __builtin_amdgcn_mfma_f32_32x32x16_bf16(__builtin_bit_cast(bf16x8, a), __builtin_bit_cast(bf16x8, b), c, 0, 0, 0);
}
DI f32x4 mfma4(unsigned a0, unsigned a1, unsigned b0, unsigned b1, f32x4 c) {
  v2u a = {a0, a1}, b = {b0, b1};
  return __builtin_amdgcn_mfma_f32_4x4x4bf16_1k(__builtin_bit_cast(s16x4, a), __builtin_bit_cast(s16x4, b), c, 0, 0, 0);
}
DI unsigned pk2(float lo, float hi) { bf16x2 v = {(__bf16)lo, (__bf16)hi}; return __builtin_bit_cast(unsigned, v); }
DI float asf(unsigned u) { return __builtin_bit_cast(float, u); }
DI unsigned asu(float f) { return __builtin_bit_cast(unsigned, f); }
DI float bflo(unsigned u) { return asf(u << 16); }
DI float bfhi(unsigned u) { return asf(u & 0xffff0000u); }
DI float halfsum(float a) {
  unsigned u = asu(a);
  auto r = __builtin_amdgcn_permlane32_swap(u, u, false, false);
  const unsigned r0 = r[0], r1 = r[1];
  return asf(r0) + asf(r1);
}
DI rsrc_t mkrs(const void* p, int bytes) { return __builtin_amdgcn_make_buffer_rsrc((void*)p, 0, bytes, 0x00020000); }
constexpr int SC1 = 16;
template <int AUX> DI v4u ld16(rsrc_t rs, int voff, int soff) { return __builtin_amdgcn_raw_buffer_load_b128(rs, voff, soff, AUX); }
template <int AUX> DI v2u ld8(rsrc_t rs, int voff, int soff) { return __builtin_amdgcn_raw_buffer_load_b64(rs, voff, soff, AUX); }
template <int AUX> DI unsigned ld4(rsrc_t rs, int voff, int soff) { return __builtin_amdgcn_raw_buffer_load_b32(rs, voff, soff, AUX); }
template <int AUX> DI void st16(v4u v, rsrc_t rs, int voff, int soff) { __builtin_amdgcn_raw_buffer_store_b128(v, rs, voff + soff, 0, AUX); }
template <int AUX> DI void st8(v2u v, rsrc_t rs, int voff, int soff) { __builtin_amdgcn_raw_buffer_store_b64(v, rs, voff, soff, AUX); }
template <int AUX> DI void st4(unsigned v, rsrc_t rs, int voff, int soff) { __builtin_amdgcn_raw_buffer_store_b32(v, rs, voff, soff, AUX); }
DI void dma16(rsrc_t rs, int voff, int soff, int lds) {
  unsigned keep;
  asm volatile("s_mov_b32 %0, m0\n\ts_mov_b32 m0, %1\n\ts_nop 0\n\tbuffer_load_dwordx4 %2, %3, %4 offen lds\n\ts_mov_b32 m0, %0"
               : "=&s"(keep) : "s"(lds), "v"(voff), "s"(rs), "s"(soff) : "memory");
}
template <int N> DI void waitv() { asm volatile("s_waitcnt vmcnt(%0)" ::"n"(N) : "memory"); }
#define BAR() asm volatile("s_waitcnt lgkmcnt(0)\n\ts_barrier" ::: "memory")
#define SB() __builtin_amdgcn_sched_barrier(0)
#define MEMFENCE() asm volatile("" ::: "memory")
DI v4u lds16(LAS char* p, int off) { return *(LAS v4u*)(p + off); }
DI v2u ldstr(LAS char* p, int off) { return __builtin_bit_cast(v2u, __builtin_amdgcn_ds_read_tr16_b64_v4bf16((LAS bf16x4*)(p + off))); }
DI v4u cat(v2u a, v2u b) { return v4u{a.x, a.y, b.x, b.y}; }
DI v4u ldsfrag_tr(LAS char* p, int off) { return cat(ldstr(p, off), ldstr(p, off + 256)); }
template <int SS> DI v4u packfrag(const f32x16& a) {
  return v4u{pk2(a[8 * SS], a[8 * SS + 1]), pk2(a[8 * SS + 2], a[8 * SS + 3]), pk2(a[8 * SS + 4], a[8 * SS + 5]), pk2(a[8 * SS + 6], a[8 * SS + 7])};
}
constexpr unsigned ONES = 0x3F803F80u, POISON = 0xFFFFFFFFu;
constexpr float LOG2E = 1.4426950408889634f;
constexpr int SPIN_CAP = 1 << 16;
#define ROT ((tm + 2 * head) % 7)
#define TOUCH 1
#define ROT1 (4 * j + (xcd & 3))
#define NW0 8
#define NW1 12
#define NW2 20
#define NW3 6
DI float clamp01(float x) { return __builtin_amdgcn_fmed3f(x, 0.f, 1.f); }
DI float phi6(float tp) { return fmaxf(tp, 1e-6f) + clamp01(__builtin_amdgcn_exp2f(tp * LOG2E)); }
DI int opaque(int v) { asm volatile("" : "+v"(v)); return v; }
DI unsigned umax3(unsigned a, unsigned b, unsigned c) { return max(max(a, b), c); }
DI unsigned vmax(unsigned m, v4u a) { return umax3(umax3(m, a.x, a.y), a.z, a.w); }
DI float gelu_f(float y) {
  constexpr float C1 = -2.f * 0.7978845608028654f * LOG2E, C2 = C1 * 0.044715f;
  const float e = __builtin_amdgcn_exp2f(y * fmaf(y * y, C2, C1));
  return y * __builtin_amdgcn_rcpf(1.f + e);
}

DI void flag_wait(unsigned f, rsrc_t rs, int voff, int base, unsigned tag) {
  if (__builtin_expect(__any(f != tag), 0)) {
    for (int spin = 0; spin < SPIN_CAP; spin++) {
      __builtin_amdgcn_s_sleep(1);
      MEMFENCE();
      if (!__any(ld4<SC1>(rs, voff, base) != tag)) break;
    }
  }
  MEMFENCE();
}

constexpr int L_W = 0, L_X = 86016, L_MISC = L_X + 65536;
constexpr int L_STT = L_MISC, L_CSX = L_MISC + 1024, L_CQX = L_MISC + 2048, L_KVF = L_MISC + 3072, L_ST2 = L_MISC + 5120, L_C1 = L_ST2 + 512;
constexpr int L_BYTES = L_C1 + 2048;
constexpr int L_RED = L_X, L_H = 0, L_HP = L_X + 49152, L_TR = 57344;

DI void transpose_unit(LAS char* stg, int wro, int rdo, v4u lo, v4u hi, v4u& f0, v4u& f1) {
  *(LAS v4u*)(stg + wro) = lo;
  *(LAS v4u*)(stg + 1024 + wro) = hi;
  f0 = cat(ldstr(stg, rdo), ldstr(stg, 1024 + rdo));
  f1 = cat(ldstr(stg, rdo ^ 64), ldstr(stg, 1024 + (rdo ^ 64)));
}

DI f32x16 reduce4(LAS char* L, int w, int vo, const f32x16& a00, const f32x16& a10, const f32x16& a01, const f32x16& a11) {
  f32x16 own;
#define PUT(T, A) \
  if (w == T) own = A; \
  else { const int sl = w - (w > T ? 1 : 0); _Pragma("unroll") for (int q = 0; q < 4; q++) \
    *(LAS f32x4*)(L + L_RED + ((T * 3 + sl) * 4 + q) * 1024 + vo) = f32x4{A[4 * q], A[4 * q + 1], A[4 * q + 2], A[4 * q + 3]}; }
  PUT(0, a00) PUT(1, a10) PUT(2, a01) PUT(3, a11)
#undef PUT
  BAR();
#pragma unroll
  for (int sl = 0; sl < 3; sl++)
#pragma unroll
    for (int q = 0; q < 4; q++) {
      const f32x4 v = *(LAS f32x4*)(L + L_RED + ((w * 3 + sl) * 4 + q) * 1024 + vo);
#pragma unroll
      for (int j = 0; j < 4; j++) own[4 * q + j] += v[j];
    }
  return own;
}

__global__ __launch_bounds__(256, 1) void fused(const u16* __restrict__ x, const u16* __restrict__ wqkv, const u16* __restrict__ wo,
                                                const u16* __restrict__ w1, const u16* __restrict__ b1, const u16* __restrict__ w2,
                                                const u16* __restrict__ b2, u16* __restrict__ out, unsigned char* __restrict__ ws) {
  extern __shared__ __attribute__((aligned(16))) char smem_[];
  LAS char* L = (LAS char*)smem_;
  const int lb = (int)(size_t)L;
  STAMP_DECL; STAMP(0);
  asm volatile("s_memtime %0\n\ts_waitcnt lgkmcnt(0)" : "=s"(ts_[30]) :: "memory");
  const int b = blockIdx.x, xcd = b & 7, idx = b >> 3, tm = 8 * (xcd >> 2) + (idx >> 2), head = 4 * (xcd & 3) + (idx & 3), odd = head & 1;
  const int lane = threadIdx.x & 63, w = __builtin_amdgcn_readfirstlane(threadIdx.x >> 6), hh = lane >> 5, r = lane & 31, vo = lane * 16;
  const rsrc_t rsX = mkrs(x, S * D * 2), rsW = mkrs(wqkv, D * 3 * D * 2), rsS = mkrs(ws, WS_END);

  const int row0 = tm * 128 + w * 32, atile = tm * 4 + w;
  const int xs = L_X + w * 16384;
  const int voffW = (lane >> 2) * 2688 + (lane & 3) * 16 + ((56 * head) & ~15);
  const int voffX0 = (row0 + (lane >> 3)) * 896 + (((lane & 7) ^ (lane >> 4)) * 16);
  const int voffX1 = (row0 + 8 + (lane >> 3)) * 896 + (((lane & 7) ^ (4 + (lane >> 4))) * 16);
  const int c0 = ROT;
#define CHUNK(i) ((c0 + (i)) >= 7 ? (c0 + (i)) - 7 : (c0 + (i)))
#define DMA_W(m, i) dma16(rsW, voffW, (m) * 896 + (4 * CHUNK(i) + w) * 43008, lb + L_W + (m) * 28672 + (4 * (i) + w) * 1024)
#define DMA_X(i, q) dma16(rsX, ((q) & 1) ? voffX1 : voffX0, ((q) >> 1) * 14336 + CHUNK(i) * 128, lb + xs + ((i) & 3) * 4096 + (q) * 1024)
#define DMA_C(i) do { DMA_W(1, i); DMA_W(2, i); DMA_X(i, 0); DMA_X(i, 1); DMA_X(i, 2); DMA_X(i, 3); } while (0)
  DMA_C(0); DMA_C(1); DMA_C(2);
  SB();
  STAMP(1);
  unsigned epoch;
  asm volatile("s_load_dword %0, %1, 0x0\n\ts_waitcnt lgkmcnt(0)" : "=s"(epoch) : "s"(ws) : "memory");
  const unsigned tag32 = epoch * 1664525u + 1013904223u, tag8 = (epoch + 1u) & 0xFFu;
  const v4u pois = {POISON, POISON, POISON, POISON};
  const bool mlp = idx < 28;
  const int g = 4 * xcd + idx / 7, j = idx % 7;
  const int btile = 2 * g + (w >> 1), ct = 2 * j + (w & 1);
  const int fa = 8 * j + 2 * w;
  const int trb = ((lane >> 5) * 8 + ((lane & 15) >> 2)) * 64 + ((lane >> 4) & 1) * 32 + (lane & 3) * 8;
  const int xrb = xs + r * 128, xsw = (r >> 1) & 7;
  f32x16 ak = {}, av = {};
  f32x4 sq = {}, sm = {}, ck = {}, cv = {};
  v4u xf[28], wk[2][4], wv[2][4], ek, ev;
#define A_READS(c) do { \
    SFOR(s4, 0, 4) \
      xf[4 * (c) + s4] = lds16(L, xrb + ((c) & 3) * 4096 + (((2 * s4 + hh) ^ xsw) * 16)); \
      wk[(c) & 1][s4] = ldsfrag_tr(L, L_W + 28672 + (4 * (c) + s4) * 1024 + trb); \
      wv[(c) & 1][s4] = ldsfrag_tr(L, L_W + 57344 + (4 * (c) + s4) * 1024 + trb); SEND \
    } while (0)
#define A_STEP(c, s4) do { const v4u xx = xf[4 * (c) + (s4)]; \
      ak = mfma32(xx, wk[(c) & 1][s4], ak); av = mfma32(xx, wv[(c) & 1][s4], av); \
      sq = mfma4(xx.x, xx.y, xx.x, xx.y, sq); sm = mfma4(ONES, ONES, xx.x, xx.y, sm); \
      sq = mfma4(xx.z, xx.w, xx.z, xx.w, sq); sm = mfma4(ONES, ONES, xx.z, xx.w, sm); } while (0)
  const int hw = ((idx - 28) * 8 + xcd) * 4 + w;
  const rsrc_t rsO_ = mkrs(mlp ? wo : w2, mlp ? D * D * 2 : D * F * 2), rs1 = mkrs(w1, D * F * 2);
  const int vrow = (lane >> 3) * 896 + (lane & 7) * 16;
  v4u rw[72];
#define PIECE(q) (hw + 128 * (q))
  int rbase[4];
#pragma unroll
  for (int q = 0; q < 4; q++) {
    const int pc = min(PIECE(q), 391), kc = (pc * 9363) >> 16;
    rbase[q] = mlp ? (28 * (4 * w + q) - 4 * (q & 1)) * 896 + j * 128 : 32 * kc * 896 + (pc - 7 * kc) * 128;
  }
  const int vlo = (mlp && lane < 32) ? OOB : vrow, vhi = (mlp && lane >= 32) ? OOB : vrow;
  const int v1 = mlp ? (lane >> 3) * 3584 + (lane & 7) * 16 + fa * 64 : OOB;
  const int r1 = ROT1;
#define W1_KS(k) ((k) + r1 >= 28 ? (k) + r1 - 28 : (k) + r1)
#define LD_RW(i) do { \
    if constexpr ((i) < 16) rw[i] = ld16<0>(rsO_, ((i) & 7) == 4 ? vlo : (((i) & 7) == 3 ? vhi : vrow), rbase[(i) >> 2] + ((i) & 3) * 8 * 896); \
    else rw[i] = ld16<0>(rs1, v1, (2 * W1_KS(((i) - 16) >> 1) + ((i) & 1)) * 8 * 3584); } while (0)
  unsigned tch = 0;
  waitv<12>(); BAR();
  STAMP(2);
  A_READS(0);
  SB();
  SFOR(c, 0, 7)
    ek = ldsfrag_tr(L, L_W + 28672 + (4 * c) * 1024 + w * 1024 + trb);
    ev = ldsfrag_tr(L, L_W + 57344 + (4 * c) * 1024 + w * 1024 + trb);
    A_STEP(c, 0);
    if (c < 4) { DMA_W(1, c + 3); DMA_W(2, c + 3); } else if (c < 6) DMA_W(0, 3 * (c - 4));
    if constexpr (c == 6) { SFOR(i, 0, NW0 / 3) LD_RW(i); SEND }
    SB();
    A_STEP(c, 1);
    if (c < 4) { DMA_X(c + 3, 0); DMA_X(c + 3, 1); } else if (c < 6) DMA_W(0, 3 * (c - 4) + 1);
    if constexpr (c == 6) { SFOR(i, NW0 / 3, 2 * NW0 / 3) LD_RW(i); SEND }
    SB();
    A_STEP(c, 2);
    if (c < 4) { DMA_X(c + 3, 2); DMA_X(c + 3, 3); } else if (c < 6) DMA_W(0, 3 * (c - 4) + 2);
    if constexpr (c == 6) { SFOR(i, 2 * NW0 / 3, NW0) LD_RW(i); SEND }
    if (TOUCH && c == 2) {
      const int t = (b * 4 + w) * 32 + r;
      const u16* tp = t < 3136 ? wo + t * 64 : (t < 15680 ? w1 + (t - 3136) * 64 : w2 + (min(t, 28223) - 15680) * 64);
      asm volatile("global_load_dword %0, %1, off" : "=v"(tch) : "v"(tp) : "memory");
    }
    ck = mfma4(ONES, ONES, ek.x, ek.y, ck); cv = mfma4(ONES, ONES, ev.x, ev.y, cv);
    ck = mfma4(ONES, ONES, ek.z, ek.w, ck); cv = mfma4(ONES, ONES, ev.z, ev.w, cv);
    SB();
    if constexpr (c < 6) {
      if (c < 2) waitv<12>(); else if (c < 4) waitv<12 + TOUCH>(); else if (c == 4) waitv<9 + TOUCH>(); else waitv<6>();
      BAR();
      A_READS(c + 1);
      SB();
    }
    A_STEP(c, 3);
    if (c == 5) DMA_W(0, 6);
    SB();
    STAMP(22 + c);
  SEND
  STAMP(4);
  float ssq = (lane & 2) ? ((lane & 1) ? sq[3] : sq[2]) : ((lane & 1) ? sq[1] : sq[0]);
  ssq = halfsum(ssq);
  const float mean = halfsum(sm[0]) * (1.f / D);
  const float var = fmaxf(ssq * (1.f / D) - mean * mean, 0.f);
  const float rs = __builtin_amdgcn_rsqf(var + 1e-5f), mrs = mean * rs;
  LAS float* stt = (LAS float*)(L + L_STT) + w * 64;
  LAS float* csx = (LAS float*)(L + L_CSX);
  {
    const float cks = halfsum(ck[0]), cvs = halfsum(cv[0]);
    stt[lane] = hh ? mrs : rs;
    csx[w * 64 + lane] = hh ? cvs : cks;
  }
  BAR();
  {
    const float csk = (csx[r] + csx[64 + r]) + (csx[128 + r] + csx[192 + r]);
    const float csv = (csx[32 + r] + csx[96 + r]) + (csx[160 + r] + csx[224 + r]);
#pragma unroll
    for (int q = 0; q < 4; q++) {
      const f32x4 r4 = *(LAS f32x4*)(stt + 8 * q + 4 * hh), m4 = *(LAS f32x4*)(stt + 32 + 8 * q + 4 * hh);
#pragma unroll
      for (int i = 0; i < 4; i++) {
        ak[4 * q + i] = phi6(fmaf(ak[4 * q + i], r4[i], fmaf(-m4[i], csk, 1e-6f)));
        av[4 * q + i] = fmaf(av[4 * q + i], r4[i], -m4[i] * csv);
      }
    }
    v4u p0 = packfrag<0>(ak), p1 = packfrag<1>(ak), v0 = packfrag<0>(av), v1 = packfrag<1>(av);
    if (r == (odd ? 0 : 28)) { v0 = v4u{ONES, ONES, ONES, ONES}; v1 = v0; }
    f32x16 kv = {};
    kv = mfma32(p0, v0, kv);
    kv = mfma32(p1, v1, kv);
    LAS char* red = L + L_X;
#pragma unroll
    for (int q = 0; q < 4; q++) *(LAS f32x4*)(red + (q * 4 + w) * 1024 + vo) = f32x4{kv[4 * q], kv[4 * q + 1], kv[4 * q + 2], kv[4 * q + 3]};
    waitv<NW0>();
    asm volatile("" :: "v"(tch));
    BAR();
    f32x4 t = *(LAS f32x4*)(red + (w * 4 + 0) * 1024 + vo) + *(LAS f32x4*)(red + (w * 4 + 1) * 1024 + vo);
    t += *(LAS f32x4*)(red + (w * 4 + 2) * 1024 + vo) + *(LAS f32x4*)(red + (w * 4 + 3) * 1024 + vo);
    v4u tu;
#pragma unroll
    for (int i = 0; i < 4; i++) tu[i] = (asu(t[i]) & ~0xFFu) | tag8;
    st16<SC1>(tu, rsS, vo, WS_KVP + ((head * 16 + tm) * 4 + w) * 1024);
    SB();
  }
  STAMP(5);
  LAS char* stg = L + L_TR + w * 6144;
  const int wro = (lane ^ ((lane >> 2) & 4)) * 16;
  const int rdo = ((lane >> 5) * 4 + ((lane & 15) >> 2)) * 128 + (((lane >> 3) & 1) * 64) + ((lane >> 4) & 1) * 32 + (lane & 3) * 8;
  v4u wo0[8], wo1[8];
  v4u wA[28], wB[28];
  f32x4 ca = {}, cb = {};
#define TR(n) do { \
    if constexpr ((n) < 8) transpose_unit(stg + ((n) % 3) * 2048, wro, rdo, rw[2 * (n)], rw[2 * (n) + 1], wo0[(n) & 7], wo1[(n) & 7]); \
    else { constexpr int k_ = (n) - 8; \
      transpose_unit(stg + ((n) % 3) * 2048, wro, rdo, rw[2 * (n)], rw[2 * (n) + 1], wA[k_], wB[k_]); \
      ca = mfma4(ONES, ONES, wA[k_].x, wA[k_].y, ca); cb = mfma4(ONES, ONES, wB[k_].x, wB[k_].y, cb); \
      ca = mfma4(ONES, ONES, wA[k_].z, wA[k_].w, ca); cb = mfma4(ONES, ONES, wB[k_].z, wB[k_].w, cb); } } while (0)
  constexpr int E1 = 16 + NW1, E2 = E1 + NW2, E3 = E2 + 8 < 72 ? E2 + 8 : 72;
  v4u pq0, pq1;
  {
    const int vow = mlp ? vo : OOB;
    st16<SC1>(pois, rsS, vo, WS_OF + (atile * 32 + 2 * head) * 1024);
    st16<SC1>(pois, rsS, vo, WS_OF + (atile * 32 + 2 * head + 1) * 1024);
    st16<SC1>(pois, rsS, vow, WS_HF + (btile * 28 + 2 * ct) * 1024);
    st16<SC1>(pois, rsS, vow, WS_HF + (btile * 28 + 2 * ct + 1) * 1024);
    st4<SC1>(POISON, rsS, mlp ? lane * 4 : OOB, WS_HST + (btile * 14 + ct) * 256);
#pragma unroll
    for (int k = 0; k < 4; k++) st16<SC1>(pois, rsS, vow, WS_H1F + ((2 * g + (k >> 1)) * 112 + 2 * fa + 2 + (k & 1)) * 1024);
    SB();
    f32x16 aq = {};
    f32x4 cq = {}, cq2 = {};
    v4u wq[4];
    SFOR(s, 0, 4) wq[s] = ldsfrag_tr(L, L_W + s * 1024 + trb); SEND
    SFOR(s, 0, 28)
      const v4u wv_ = wq[s & 3];
      aq = mfma32(wv_, xf[s], aq);
      cq = mfma4(ONES, ONES, wv_.x, wv_.y, cq);
      cq2 = mfma4(ONES, ONES, wv_.z, wv_.w, cq2);
      if constexpr (s + 4 < 28) wq[s & 3] = ldsfrag_tr(L, L_W + (s + 4) * 1024 + trb);
      if constexpr (NW0 + s < E1) LD_RW(NW0 + s);
      if constexpr (s >= 12 && (s & 3) == 0 && (s - 12) / 4 < NW0 / 2) TR((s - 12) / 4);
      SB();
    SEND
    LAS float* cqx = (LAS float*)(L + L_CQX) + w * 64;
    cqx[lane] = halfsum(cq[0] + cq2[0]);
#pragma unroll
    for (int q = 0; q < 4; q++) {
      const f32x4 c4 = *(LAS f32x4*)(cqx + 8 * q + 4 * hh);
#pragma unroll
      for (int i = 0; i < 4; i++) aq[4 * q + i] = phi6(fmaf(aq[4 * q + i], rs, fmaf(-mrs, c4[i], 1e-6f)));
    }
    pq0 = packfrag<0>(aq); pq1 = packfrag<1>(aq);
  }
  SB();
  waitv<E1 - NW0>();
  st4<SC1>(tag32, rsS, lane == 0 ? WS_FLAG2 + (atile >> 1) * 256 + ((atile & 1) * 16 + head) * 4 : (lane == 1 && mlp ? WS_FLAG2 + g * 256 + (32 + j * 4 + w) * 4 : OOB), 0);
  STAMP(6);
  {
    f32x4 t;
    v4u p[16];
#define KV_LOADS() _Pragma("unroll") for (int i = 0; i < 16; i++) p[i] = ld16<SC1>(rsS, vo, WS_KVP + ((head * 16 + i) * 4 + w) * 1024)
#define KV_CHECK() unsigned bad = 0; t = f32x4{0.f, 0.f, 0.f, 0.f}; \
      _Pragma("unroll") for (int i = 0; i < 16; i++) { \
        const v4u y = p[i] ^ tag8;    \
        bad |= y.x | y.y; bad |= y.z | y.w; \
        t += __builtin_bit_cast(f32x4, y); } \
      const bool again = __any((bad & 0xFFu) != 0)
    KV_LOADS();
    SB();
    SFOR(n, (NW0 / 2 < 4 ? NW0 / 2 : 4), E1 / 2) TR(n); SEND
    SB();
    {
      KV_CHECK();
      if (again) {
        for (int spin = 0; spin < SPIN_CAP; spin++) {
          MEMFENCE();
          KV_LOADS();
          SB();
          KV_CHECK();
          if (!again) break;
          __builtin_amdgcn_s_sleep(2);
        }
      }
    }
    STAMP(7);
    v2u o2 = {pk2(t[0], t[1]), pk2(t[2], t[3])};
    if (odd ? (w == 0 && hh == 0) : (w == 3 && hh == 1)) o2 = v2u{0, 0};
    *(LAS v2u*)(L + L_KVF + (w >> 1) * 1024 + vo + (w & 1) * 8) = o2;
    BAR();
    const v4u k0 = lds16(L, L_KVF + vo), k1 = lds16(L, L_KVF + 1024 + vo);
    f32x16 o = {};
    o = mfma32(k0, pq0, o);
    o = mfma32(k1, pq1, o);
    const unsigned tu = asu(odd ? o[0] : o[12]);
    auto sw = __builtin_amdgcn_permlane32_swap(tu, tu, false, false);
    const unsigned s0 = sw[0], s1 = sw[1];
    const float inv = __builtin_amdgcn_rcpf(asf(odd ? s0 : s1) + 1e-6f);
#pragma unroll
    for (int i = 0; i < 16; i++) o[i] *= inv;
    st16<SC1>(packfrag<0>(o), rsS, vo, WS_OF + (atile * 32 + 2 * head) * 1024);
    st16<SC1>(packfrag<1>(o), rsS, vo, WS_OF + (atile * 32 + 2 * head + 1) * 1024);
    SB();
  }
  STAMP(8);
  if (!mlp) {
    SFOR(u, 0, 8)
      const int pc = PIECE(u >> 1), kc = (pc * 9363) >> 16, cb_ = pc - 7 * kc;
      const int dst = pc < 392 ? WS_W2T + (2 * cb_ * 112 + 2 * kc + (u & 1)) * 1024 : OOB;
      st16<SC1>(wo0[u], rsS, vo, dst);
      st16<SC1>(wo1[u], rsS, vo, dst + 112 * 1024);
    SEND
    waitv<0>();
    st4<SC1>(tag32, rsS, lane == 0 ? WS_FLAG1 + hw * 4 : OOB, 0);
    STAMP_OUT(0, 31);
    return;
  }
  unsigned f3;
  v4u u0[28], u1[28];
#define D_KS(i) (28 * w + ((i) < 14 ? 4 * ((i) >> 1) + ((i) & 1) : 4 * (((i) - 14) >> 1) + 2 + ((i) & 1)))
#define LD_U(q) do { if constexpr ((q) & 1) u1[(q) >> 1] = ld16<SC1>(rsS, vo, WS_W2T + ((2 * j + 1) * 112 + D_KS((q) >> 1)) * 1024); \
                     else u0[(q) >> 1] = ld16<SC1>(rsS, vo, WS_W2T + ((2 * j) * 112 + D_KS((q) >> 1)) * 1024); } while (0)
  {
    const int fo = (lane < 60 ? lane : 0) * 4;
    const unsigned f2 = ld4<SC1>(rsS, fo, WS_FLAG2 + g * 256);
    v2u xr[4];
#pragma unroll
    for (int q = 0; q < 4; q++) xr[q] = ld8<0>(rsX, (btile * 32 + r) * 896 + hh * 8, ct * 64 + q * 16);
    SFOR(i, E1, E2) LD_RW(i); SEND
    SB();
    flag_wait(f2, rsS, fo, WS_FLAG2 + g * 256, tag32);
    STAMP(9);
    v4u o0[8], o1[8];
#define O_LOADS() _Pragma("unroll") for (int i = 0; i < 8; i++) { \
        o0[i] = ld16<SC1>(rsS, vo, WS_OF + ((2 * g) * 32 + 8 * w + i) * 1024); \
        o1[i] = ld16<SC1>(rsS, vo, WS_OF + ((2 * g + 1) * 32 + 8 * w + i) * 1024); }
#define O_CHECK() unsigned m = 0; _Pragma("unroll") for (int i = 0; i < 8; i++) m = vmax(vmax(m, o0[i]), o1[i]); const bool again = __any(m == POISON)
    O_LOADS();
    SB();
    SFOR(n, E1 / 2, E2 / 2) TR(n); SEND
    SB();
    {
      O_CHECK();
      if (again) {
        for (int spin = 0; spin < SPIN_CAP; spin++) {
          MEMFENCE();
          O_LOADS();
          SB();
          O_CHECK();
          if (!again) break;
          __builtin_amdgcn_s_sleep(2);
        }
      }
    }
    STAMP(10);
    if (b == 0 && threadIdx.x == 0) st4<SC1>(tag32, rsS, 0, WS_EPOCH);
    f32x16 a00 = {}, a10 = {}, a01 = {}, a11 = {};
    SFOR(i, 0, 8)
      a00 = mfma32(wo0[i], o0[i], a00); a10 = mfma32(wo1[i], o0[i], a10);
      a01 = mfma32(wo0[i], o1[i], a01); a11 = mfma32(wo1[i], o1[i], a11);
      if constexpr (E2 + i < E3) LD_RW(E2 + i);
      SB();
    SEND
    const f32x16 a = reduce4(L, w, opaque(vo), a00, a10, a01, a11);
    unsigned hp[8];
#pragma unroll
    for (int q = 0; q < 4; q++) {
      hp[2 * q] = pk2(a[4 * q] + bflo(xr[q].x), a[4 * q + 1] + bfhi(xr[q].x));
      hp[2 * q + 1] = pk2(a[4 * q + 2] + bflo(xr[q].y), a[4 * q + 3] + bfhi(xr[q].y));
    }
    st16<SC1>(v4u{hp[0], hp[1], hp[2], hp[3]}, rsS, vo, WS_HF + (btile * 28 + 2 * ct) * 1024);
    st16<SC1>(v4u{hp[4], hp[5], hp[6], hp[7]}, rsS, vo, WS_HF + (btile * 28 + 2 * ct + 1) * 1024);
    float s1 = 0.f, s2 = 0.f;
#pragma unroll
    for (int q = 0; q < 8; q++) { const float lo = bflo(hp[q]), hi = bfhi(hp[q]); s1 += lo + hi; s2 = fmaf(lo, lo, fmaf(hi, hi, s2)); }
    s1 = halfsum(s1); s2 = halfsum(s2);
    if (hh == 0) st8<SC1>(v2u{asu(s1), asu(s2)}, rsS, r * 8, WS_HST + (btile * 14 + ct) * 256);
    *(LAS v4u*)(L + L_HP + w * 2048 + vo) = v4u{hp[0], hp[1], hp[2], hp[3]};
    *(LAS v4u*)(L + L_HP + w * 2048 + 1024 + vo) = v4u{hp[4], hp[5], hp[6], hp[7]};
    SB();
  }
  STAMP(11);
  {
    const int f1o = (((14 * w + (lane < 14 ? lane : 0)) * 7 + j) & 127) * 4;
    const unsigned f1 = ld4<SC1>(rsS, f1o, WS_FLAG1);
    const float bv = bflo(b1[fa * 32 + lane]);
    SFOR(i, E3, 72) LD_RW(i); SEND
    SB();
    flag_wait(f1, rsS, f1o, WS_FLAG1, tag32);
    SFOR(q, 0, NW3) LD_U(q); SEND
    LAS float* c1x = (LAS float*)(L + L_C1) + w * 128;
    {
      v4u hr[14]; v2u st[7];
#define H_LOADS() _Pragma("unroll") for (int i = 0; i < 14; i++) hr[i] = ld16<SC1>(rsS, vo, WS_HF + ((2 * g) * 28 + 14 * w + i) * 1024); \
        _Pragma("unroll") for (int i = 0; i < 7; i++) st[i] = ld8<SC1>(rsS, lane * 8, WS_HST + ((2 * g + (w & 1)) * 14 + 2 * i) * 256)
#define H_CHECK() unsigned m = 0; _Pragma("unroll") for (int i = 0; i < 14; i++) m = vmax(m, hr[i]); \
        _Pragma("unroll") for (int i = 0; i < 7; i++) m = umax3(m, st[i].x, st[i].y); const bool again = __any(m == POISON)
      H_LOADS();
      SB();
      SFOR(n, E2 / 2, 36) TR(n); SEND
      {
        const float sa = halfsum(ca[0]), sb = halfsum(cb[0]);
        c1x[lane] = hh ? sb : sa; c1x[64 + lane] = bv;
      }
      SB();
      {
        H_CHECK();
        if (again) {
          for (int spin = 0; spin < SPIN_CAP; spin++) {
            MEMFENCE();
            H_LOADS();
            SB();
            H_CHECK();
            if (!again) break;
            __builtin_amdgcn_s_sleep(2);
          }
        }
      }
      STAMP(12);
#pragma unroll
      for (int i = 0; i < 14; i++) {
        const int ks = 14 * (w & 1) + i - r1;
        *(LAS v4u*)(L + L_H + ((w >> 1) * 28 + (ks < 0 ? ks + 28 : ks)) * 1024 + vo) = hr[i];
      }
      float s1 = 0.f, s2 = 0.f;
#pragma unroll
      for (int i = 0; i < 7; i++) { s1 += asf(st[i].x); s2 += asf(st[i].y); }
      s1 = halfsum(s1); s2 = halfsum(s2);
      const float mean2 = s1 * (1.f / D), var2 = fmaxf(s2 * (1.f / D) - mean2 * mean2, 0.f);
      const float rs2 = __builtin_amdgcn_rsqf(var2 + 1e-5f);
      if (w < 2 && hh == 0) *(LAS f32x2*)(L + L_ST2 + w * 256 + r * 8) = f32x2{rs2, -mean2 * rs2};
    }
    BAR();
    STAMP(13);
    const f32x2 sv0 = *(LAS f32x2*)(L + L_ST2 + r * 8), sv1 = *(LAS f32x2*)(L + L_ST2 + 256 + r * 8);
    constexpr int LA = 6;
    v4u hf[LA];
    SFOR(i, 0, LA) hf[i] = lds16(L, L_H + i * 1024 + vo); SEND
    f32x16 acc[2];
    f32x4 cc[2], bb[2];
    unsigned pkd[8];
    float ya[2], eb[2];
    SFOR(T, 0, 5)
      constexpr int P = T - 1;
      if constexpr (T < 4) acc[T & 1] = f32x16{};
      SFOR(i, 0, 28)
        if constexpr (T < 4) {
          constexpr int n = T * 28 + i, nn = n + LA;
          acc[T & 1] = mfma32(T < 2 ? wA[i] : wB[i], hf[n % LA], acc[T & 1]);
          if constexpr (nn < 112) hf[n % LA] = lds16(L, L_H + (((nn / 28) & 1) * 28 + nn % 28) * 1024 + vo);
          if constexpr (n < 56) { if constexpr ((n & 1) == 0 && NW3 + n / 2 < 28) LD_U(NW3 + n / 2); }
          else if constexpr ((n & 1) == 0) LD_U(28 + ((n - 56) >> 1));
        }
        if constexpr (i % 7 == 3) {
          constexpr int nq = (T * 4 + i / 7 + 1), nP = nq / 4 - 1;
          if constexpr (nP >= 0 && nP < 4) {
            cc[nq & 1] = *(LAS f32x4*)(c1x + (nP >> 1) * 32 + 8 * (nq & 3) + 4 * hh);
            bb[nq & 1] = *(LAS f32x4*)(c1x + 64 + (nP >> 1) * 32 + 8 * (nq & 3) + 4 * hh);
          }
        }
        if constexpr (T > 0) {
          const f32x2 sv = (P & 1) ? sv1 : sv0;
          constexpr int m7 = i % 7, pr = 2 * (i / 7) + (m7 >= 3 ? 1 : 0), stage = m7 >= 3 ? m7 - 3 : m7;
          constexpr float C1 = -2.f * 0.7978845608028654f * LOG2E, C2 = C1 * 0.044715f;
          if constexpr (m7 < 6) {
            SFOR(e, 0, 2)
              constexpr int reg = 2 * pr + e;
              if constexpr (stage == 0) {
                ya[e] = fmaf(acc[P & 1][reg], sv.x, fmaf(sv.y, cc[(reg >> 2) & 1][reg & 3], bb[(reg >> 2) & 1][reg & 3]));
                eb[e] = fmaf(ya[e] * ya[e], C2, C1);
              } else if constexpr (stage == 1) {
                eb[e] = 1.f + __builtin_amdgcn_exp2f(ya[e] * eb[e]);
              } else {
                ya[e] = ya[e] * __builtin_amdgcn_rcpf(eb[e]);
              }
            SEND
            if constexpr (stage == 2) pkd[pr] = pk2(ya[0], ya[1]);
            else if constexpr (T < 4) asm volatile("" : "+v"(ya[0]), "+v"(ya[1]), "+v"(eb[0]), "+v"(eb[1]));
          }
          if constexpr (i == 13) st16<SC1>(v4u{pkd[0], pkd[1], pkd[2], pkd[3]}, rsS, vo, WS_H1F + ((2 * g + (P & 1)) * 112 + 2 * (fa + (P >> 1))) * 1024);
          if constexpr (i == 27) st16<SC1>(v4u{pkd[4], pkd[5], pkd[6], pkd[7]}, rsS, vo, WS_H1F + ((2 * g + (P & 1)) * 112 + 2 * (fa + (P >> 1)) + 1) * 1024);
        }
        if constexpr (T == 3 && i == 12) {
          waitv<7>();
          st4<SC1>(tag32, rsS, lane == 0 ? WS_FLAG3 + (g * 32 + j * 4 + w) * 4 : OOB, 0);
        }
        if constexpr (T == 4 && i == 0) f3 = ld4<SC1>(rsS, (r < 28 ? r : 0) * 4, WS_FLAG3 + g * 128);
        if constexpr (T < 4) SB();
      SEND
      if constexpr (T == 1) STAMP(14);
      if constexpr (T == 3) STAMP(15);
    SEND
    SB();
  }
  STAMP(16);
  {
    const rsrc_t rsB = mkrs(b2, D * 2), rsO = mkrs(out, S * D * 2);
    v2u br[4];
#pragma unroll
    for (int q = 0; q < 4; q++) br[q] = ld8<0>(rsB, hh * 8, ct * 64 + q * 16);
    flag_wait(f3, rsS, (r < 28 ? r : 0) * 4, WS_FLAG3 + g * 128, tag32);
    constexpr int WIN = 12;
    v4u x0[WIN], x1[WIN];
    const int hb0 = WS_H1F + (2 * g) * 112 * 1024, hb1 = hb0 + 112 * 1024;
    SFOR(i, 0, WIN) x0[i] = ld16<SC1>(rsS, vo, hb0 + D_KS(i) * 1024); x1[i] = ld16<SC1>(rsS, vo, hb1 + D_KS(i) * 1024); SEND
    SB();
    f32x16 a00 = {}, a10 = {}, a01 = {}, a11 = {};
    SFOR(i, 0, 28)
      v4u y0 = x0[i % WIN], y1 = x1[i % WIN];
      if (i >= 14 && __builtin_expect(__any(vmax(vmax(0u, y0), y1) == POISON), 0)) {
        for (int spin = 0; spin < SPIN_CAP; spin++) {
          __builtin_amdgcn_s_sleep(1);
          MEMFENCE();
          y0 = ld16<SC1>(rsS, vo, hb0 + D_KS(i) * 1024); y1 = ld16<SC1>(rsS, vo, hb1 + D_KS(i) * 1024);
          if (!__any(vmax(vmax(0u, y0), y1) == POISON)) break;
        }
      }
      a00 = mfma32(u0[i], y0, a00); a10 = mfma32(u1[i], y0, a10);
      a01 = mfma32(u0[i], y1, a01); a11 = mfma32(u1[i], y1, a11);
      if constexpr (i + WIN < 28) { x0[i % WIN] = ld16<SC1>(rsS, vo, hb0 + D_KS(i + WIN) * 1024); x1[i % WIN] = ld16<SC1>(rsS, vo, hb1 + D_KS(i + WIN) * 1024); }
      SB();
      if constexpr (i == 0) STAMP(17);
      if constexpr (i == 13) STAMP(18);
    SEND
    STAMP(19);
    const int vd = opaque(vo);
    const f32x16 a = reduce4(L, w, vd, a00, a10, a01, a11);
    const v4u hq0 = lds16(L, L_HP + w * 2048 + vd), hq1 = lds16(L, L_HP + w * 2048 + 1024 + vd);
    const unsigned hp[8] = {hq0.x, hq0.y, hq0.z, hq0.w, hq1.x, hq1.y, hq1.z, hq1.w};
#pragma unroll
    for (int q = 0; q < 4; q++) {
      v2u o;
      o.x = pk2(a[4 * q] + bflo(br[q].x) + bflo(hp[2 * q]), a[4 * q + 1] + bfhi(br[q].x) + bfhi(hp[2 * q]));
      o.y = pk2(a[4 * q + 2] + bflo(br[q].y) + bflo(hp[2 * q + 1]), a[4 * q + 3] + bfhi(br[q].y) + bfhi(hp[2 * q + 1]));
      st8<0>(o, rsO, (btile * 32 + ((vd >> 4) & 31)) * 896 + (vd >> 9) * 8, ct * 64 + q * 16);
    }
  }
  STAMP(20); waitv<0>(); STAMP(21); asm volatile("s_memtime %0\n\ts_waitcnt lgkmcnt(0)" : "=s"(ts_[31]) :: "memory"); STAMP_OUT(0, 31);
}

__global__ void k_report(unsigned char* ws) {
  __shared__ unsigned long long sa[256], sb[256];
  const unsigned long long* sp = (const unsigned long long*)(ws + WS_STAMP);
  const int t = threadIdx.x;
  const bool valid = (t >> 3) < 28;
  sa[t] = valid ? sp[t * 32 + 0] : sp[0]; sb[t] = valid ? sp[t * 32 + 0] : sp[0];
  __syncthreads();
  unsigned long long a = sa[0], b = sb[0];
  for (int k = 1; k < 256; k++) { a = min(a, sa[k]); b = min(b, sb[k]); }
  unsigned long long n = (b - a) * 4ull;
  if (n > 20000ull) n = 20000ull;
  unsigned long long t0 = __builtin_amdgcn_s_memrealtime();
  while (__builtin_amdgcn_s_memrealtime() - t0 < n) __builtin_amdgcn_s_sleep(1);
}
extern "C" void kernel_launch(void* const* d_in, const int* in_sizes, int n_in, void* d_out, int out_size, void* d_ws, size_t ws_size,
                              hipStream_t stream) {
  (void)hipFuncSetAttribute((const void*)fused, hipFuncAttributeMaxDynamicSharedMemorySize, L_BYTES);
  fused<<<256, 256, L_BYTES, stream>>>((const u16*)d_in[0], (const u16*)d_in[1], (const u16*)d_in[2], (const u16*)d_in[4], (const u16*)d_in[5],
                                       (const u16*)d_in[6], (const u16*)d_in[7], (u16*)d_out, (unsigned char*)d_ws);
  k_report<<<1, 256, 0, stream>>>((unsigned char*)d_ws);
}
```

```cpp
#include <hip/hip_runtime.h>
#include <hip/hip_bf16.h>
#include <stdint.h>

typedef unsigned short u16;
typedef unsigned v4u __attribute__((ext_vector_type(4)));
typedef unsigned v2u __attribute__((ext_vector_type(2)));
typedef float f32x2 __attribute__((ext_vector_type(2)));
typedef float f32x4 __attribute__((ext_vector_type(4)));
typedef float f32x16 __attribute__((ext_vector_type(16)));
typedef __bf16 bf16x8 __attribute__((ext_vector_type(8)));
typedef __bf16 bf16x4 __attribute__((ext_vector_type(4)));
typedef __bf16 bf16x2 __attribute__((ext_vector_type(2)));
typedef short s16x4 __attribute__((ext_vector_type(4)));
#define LAS __attribute__((address_space(3)))
#define DI __device__ __forceinline__
typedef __amdgpu_buffer_rsrc_t rsrc_t;

constexpr int S = 2048, D = 448, F = 1792;
constexpr int WS_EPOCH = 0;
constexpr int WS_FLAG1 = 4096;
constexpr int WS_FLAG2 = 8192;
constexpr int WS_FLAG3 = 16384;
constexpr int WS_KVP = 65536;
constexpr int WS_OF = WS_KVP + 16 * 16 * 4096;
constexpr int WS_HF = WS_OF + 64 * 32 * 1024;
constexpr int WS_HST = WS_HF + 64 * 28 * 1024;
constexpr int WS_H1F = WS_HST + 64 * 14 * 256;
constexpr int WS_W2T = WS_H1F + 64 * 112 * 1024;
constexpr int WS_END = WS_W2T + 14 * 112 * 1024;
constexpr int OOB = 0x7ff00000;
#define STAMP(i)
#define STAMP_DECL
#define STAMP_OUT(lo, hi)

template <int I> struct IC { static constexpr int v = I; constexpr operator int() const { return I; } };
template <int LO, int HI, class Fn> DI void sfor(Fn&& fn) {
  if constexpr (LO < HI) { fn(IC<LO>{}); sfor<LO + 1, HI>(fn); }
}
#define SFOR(i, lo, hi) sfor<(lo), (hi)>([&](auto i##_) __attribute__((always_inline)) { constexpr int i = decltype(i##_)::v;
#define SEND });

DI f32x16 mfma32(v4u a, v4u b, f32x16 c) {
  return __builtin_amdgcn_mfma_f32_32x32x16_bf16(__builtin_bit_cast(bf16x8, a), __builtin_bit_cast(bf16x8, b), c, 0, 0, 0);
}
DI f32x4 mfma4(unsigned a0, unsigned a1, unsigned b0, unsigned b1, f32x4 c) {
  v2u a = {a0, a1}, b = {b0, b1};
  return __builtin_amdgcn_mfma_f32_4x4x4bf16_1k(__builtin_bit_cast(s16x4, a), __builtin_bit_cast(s16x4, b), c, 0, 0, 0);
}
DI unsigned pk2(float lo, float hi) { bf16x2 v = {(__bf16)lo, (__bf16)hi}; return __builtin_bit_cast(unsigned, v); }
DI float asf(unsigned u) { return __builtin_bit_cast(float, u); }
DI unsigned asu(float f) { return __builtin_bit_cast(unsigned, f); }
DI float bflo(unsigned u) { return asf(u << 16); }
DI float bfhi(unsigned u) { return asf(u & 0xffff0000u); }
DI float halfsum(float a) {
  unsigned u = asu(a);
  auto r = __builtin_amdgcn_permlane32_swap(u, u, false, false);
  const unsigned r0 = r[0], r1 = r[1];
  return asf(r0) + asf(r1);
}
DI rsrc_t mkrs(const void* p, int bytes) { return __builtin_amdgcn_make_buffer_rsrc((void*)p, 0, bytes, 0x00020000); }
constexpr int SC1 = 16;
template <int AUX> DI v4u ld16(rsrc_t rs, int voff, int soff) { return __builtin_amdgcn_raw_buffer_load_b128(rs, voff, soff, AUX); }
template <int AUX> DI v2u ld8(rsrc_t rs, int voff, int soff) { return __builtin_amdgcn_raw_buffer_load_b64(rs, voff, soff, AUX); }
template <int AUX> DI unsigned ld4(rsrc_t rs, int voff, int soff) { return __builtin_amdgcn_raw_buffer_load_b32(rs, voff, soff, AUX); }
template <int AUX> DI void st16(v4u v, rsrc_t rs, int voff, int soff) { __builtin_amdgcn_raw_buffer_store_b128(v, rs, voff + soff, 0, AUX); }
template <int AUX> DI void st8(v2u v, rsrc_t rs, int voff, int soff) { __builtin_amdgcn_raw_buffer_store_b64(v, rs, voff, soff, AUX); }
template <int AUX> DI void st4(unsigned v, rsrc_t rs, int voff, int soff) { __builtin_amdgcn_raw_buffer_store_b32(v, rs, voff, soff, AUX); }
DI void dma16(rsrc_t rs, int voff, int soff, int lds) {
  unsigned keep;
  asm volatile("s_mov_b32 %0, m0\n\ts_mov_b32 m0, %1\n\ts_nop 0\n\tbuffer_load_dwordx4 %2, %3, %4 offen lds\n\ts_mov_b32 m0, %0"
               : "=&s"(keep) : "s"(lds), "v"(voff), "s"(rs), "s"(soff) : "memory");
}
DI void dma16s(rsrc_t rs, int voff, int soff, int lds) {
  unsigned keep;
  asm volatile("s_mov_b32 %0, m0\n\ts_mov_b32 m0, %1\n\ts_nop 0\n\tbuffer_load_dwordx4 %2, %3, %4 offen sc1 lds\n\ts_mov_b32 m0, %0"
               : "=&s"(keep) : "s"(lds), "v"(voff), "s"(rs), "s"(soff) : "memory");
}
template <int N> DI void waitv() { asm volatile("s_waitcnt vmcnt(%0)" ::"n"(N) : "memory"); }
#define BAR() asm volatile("s_waitcnt lgkmcnt(0)\n\ts_barrier" ::: "memory")
#define SB() __builtin_amdgcn_sched_barrier(0)
#define MEMFENCE() asm volatile("" ::: "memory")
DI v4u lds16(LAS char* p, int off) { return *(LAS v4u*)(p + off); }
DI v2u ldstr(LAS char* p, int off) { return __builtin_bit_cast(v2u, __builtin_amdgcn_ds_read_tr16_b64_v4bf16((LAS bf16x4*)(p + off))); }
DI v4u cat(v2u a, v2u b) { return v4u{a.x, a.y, b.x, b.y}; }
DI v4u ldsfrag_tr(LAS char* p, int off) { return cat(ldstr(p, off), ldstr(p, off + 256)); }
template <int SS> DI v4u packfrag(const f32x16& a) {
  return v4u{pk2(a[8 * SS], a[8 * SS + 1]), pk2(a[8 * SS + 2], a[8 * SS + 3]), pk2(a[8 * SS + 4], a[8 * SS + 5]), pk2(a[8 * SS + 6], a[8 * SS + 7])};
}
constexpr unsigned ONES = 0x3F803F80u, POISON = 0xFFFFFFFFu;
constexpr float LOG2E = 1.4426950408889634f;
constexpr int SPIN_CAP = 1 << 16;
#define ROT ((tm + 2 * head) % 7)
#define TOUCH 1
#define ROT1 (4 * j + (xcd & 3))
#define NW0 10
#define NW1 10
#define NW2 16
#define NQ 10
#define NWB 12
#define NT3 3
#define NUP 12
#define NUR 24
#define QP 12
DI float clamp01(float x) { return __builtin_amdgcn_fmed3f(x, 0.f, 1.f); }
DI float phi6(float tp) { return fmaxf(tp, 1e-6f) + clamp01(__builtin_amdgcn_exp2f(tp * LOG2E)); }
DI bool anyl(bool p) { return __builtin_amdgcn_ballot_w64(p) != 0; }
DI void pin4(float& a, float& b, float& c, float& d) { asm volatile("" : "+v"(a), "+v"(b), "+v"(c), "+v"(d)); }
DI int opaque(int v) { asm volatile("" : "+v"(v)); return v; }
DI unsigned umax3(unsigned a, unsigned b, unsigned c) { return max(max(a, b), c); }
DI unsigned vmax(unsigned m, v4u a) { return umax3(umax3(m, a.x, a.y), a.z, a.w); }
DI float gelu_f(float y) {
  constexpr float C1 = -2.f * 0.7978845608028654f * LOG2E, C2 = C1 * 0.044715f;
  const float e = __builtin_amdgcn_exp2f(y * fmaf(y * y, C2, C1));
  return y * __builtin_amdgcn_rcpf(1.f + e);
}

DI void flag_wait(unsigned f, rsrc_t rs, int voff, int base, unsigned tag) {
  if (__builtin_expect(anyl(f != tag), 0)) {
    for (int spin = 0; spin < SPIN_CAP; spin++) {
      __builtin_amdgcn_s_sleep(1);
      MEMFENCE();
      if (!anyl(ld4<SC1>(rs, voff, base) != tag)) break;
    }
  }
  MEMFENCE();
}

constexpr int L_W = 0, L_X = 86016, L_MISC = L_X + 65536;
constexpr int L_STT = L_MISC, L_CSX = L_MISC + 1024, L_CQX = L_MISC + 2048, L_KVF = L_MISC + 3072, L_ST2 = L_MISC + 5120, L_C1 = L_ST2 + 512;
constexpr int L_BYTES = L_C1 + 2048;
constexpr int L_RED = L_X, L_H = 0, L_HP = L_X + 49152, L_TR = 57344;

DI void transpose_unit(LAS char* stg, int wro, int rdo, v4u lo, v4u hi, v4u& f0, v4u& f1) {
  *(LAS v4u*)(stg + wro) = lo;
  *(LAS v4u*)(stg + 1024 + wro) = hi;
  f0 = cat(ldstr(stg, rdo), ldstr(stg, 1024 + rdo));
  f1 = cat(ldstr(stg, rdo ^ 64), ldstr(stg, 1024 + (rdo ^ 64)));
}

DI f32x16 reduce4(LAS char* L, int w, int vo, const f32x16& a00, const f32x16& a10, const f32x16& a01, const f32x16& a11) {
  f32x16 own;
#define PUT(T, A) \
  if (w == T) own = A; \
  else { const int sl = w - (w > T ? 1 : 0); _Pragma("unroll") for (int q = 0; q < 4; q++) \
    *(LAS f32x4*)(L + L_RED + ((T * 3 + sl) * 4 + q) * 1024 + vo) = f32x4{A[4 * q], A[4 * q + 1], A[4 * q + 2], A[4 * q + 3]}; }
  PUT(0, a00) PUT(1, a10) PUT(2, a01) PUT(3, a11)
#undef PUT
  BAR();
#pragma unroll
  for (int sl = 0; sl < 3; sl++)
#pragma unroll
    for (int q = 0; q < 4; q++) {
      const f32x4 v = *(LAS f32x4*)(L + L_RED + ((w * 3 + sl) * 4 + q) * 1024 + vo);
#pragma unroll
      for (int j = 0; j < 4; j++) own[4 * q + j] += v[j];
    }
  return own;
}

__global__ __launch_bounds__(256, 1) void fused(const u16* __restrict__ x, const u16* __restrict__ wqkv, const u16* __restrict__ wo,
                                                const u16* __restrict__ w1, const u16* __restrict__ b1, const u16* __restrict__ w2,
                                                const u16* __restrict__ b2, u16* __restrict__ out, unsigned char* __restrict__ ws) {
  extern __shared__ __attribute__((aligned(16))) char smem_[];
  LAS char* L = (LAS char*)smem_;
  const int lb = (int)(size_t)L;
  STAMP_DECL; STAMP(0);
  const int b = blockIdx.x, xcd = b & 7, idx = b >> 3, tm = 8 * (xcd >> 2) + (idx >> 2), head = 4 * (xcd & 3) + (idx & 3), odd = head & 1;
  const int lane = threadIdx.x & 63, w = __builtin_amdgcn_readfirstlane(threadIdx.x >> 6), hh = lane >> 5, r = lane & 31, vo = lane * 16;
  const rsrc_t rsX = mkrs(x, S * D * 2), rsW = mkrs(wqkv, D * 3 * D * 2), rsS = mkrs(ws, WS_END);

  const int row0 = tm * 128 + w * 32, atile = tm * 4 + w;
  const int xs = L_X + w * 16384;
  const int voffW = (lane >> 2) * 2688 + (lane & 3) * 16 + ((56 * head) & ~15);
  const int voffX0 = (row0 + (lane >> 3)) * 896 + (((lane & 7) ^ (lane >> 4)) * 16);
  const int voffX1 = (row0 + 8 + (lane >> 3)) * 896 + (((lane & 7) ^ (4 + (lane >> 4))) * 16);
  const int c0 = ROT;
#define CHUNK(i) ((c0 + (i)) >= 7 ? (c0 + (i)) - 7 : (c0 + (i)))
#define DMA_W(m, i) dma16(rsW, voffW, (m) * 896 + (4 * CHUNK(i) + w) * 43008, lb + L_W + (m) * 28672 + (4 * (i) + w) * 1024)
#define DMA_X(i, q) dma16(rsX, ((q) & 1) ? voffX1 : voffX0, ((q) >> 1) * 14336 + CHUNK(i) * 128, lb + xs + ((i) & 3) * 4096 + (q) * 1024)
#define DMA_C(i) do { DMA_W(1, i); DMA_W(2, i); DMA_X(i, 0); DMA_X(i, 1); DMA_X(i, 2); DMA_X(i, 3); } while (0)
  unsigned ctch;
  {
    unsigned long long pc; unsigned len;
    asm volatile("fused_code_begin_%=:\n\ts_getpc_b64 %0\n\ts_mov_b32 %1, .Lfunc_end0 - fused_code_begin_%=" : "=s"(pc), "=s"(len));
    const unsigned off = min((unsigned)(((idx * 4 + w) * 4 + (lane & 3)) * 128), len - 8u) & ~3u;
    asm volatile("global_load_dword %0, %1, %2" : "=v"(ctch) : "v"(off), "s"(pc) : "memory");
  }
  DMA_C(0); DMA_C(1); DMA_C(2);
  SB();
  STAMP(1);
  unsigned epoch;
  asm volatile("s_load_dword %0, %1, 0x0\n\ts_waitcnt lgkmcnt(0)" : "=s"(epoch) : "s"(ws) : "memory");
  const unsigned tag32 = epoch * 1664525u + 1013904223u, tag8 = (epoch + 1u) & 0xFFu;
  const v4u pois = {POISON, POISON, POISON, POISON};
  const bool mlp = idx < 28;
  const int g = 4 * xcd + idx / 7, j = idx % 7;
  const int btile = 2 * g + (w >> 1), ct = 2 * j + (w & 1);
  const int fa = 8 * j + 2 * w;
  const int trb = ((lane >> 5) * 8 + ((lane & 15) >> 2)) * 64 + ((lane >> 4) & 1) * 32 + (lane & 3) * 8;
  const int xrb = xs + r * 128, xsw = (r >> 1) & 7;
  f32x16 ak = {}, av = {};
  f32x4 sq = {}, sm = {}, ck = {}, cv = {};
  v4u xf[28], wk[2][4], wv[2][4], ek, ev;
#define A_READS(c) do { \
    SFOR(s4, 0, 4) \
      xf[4 * (c) + s4] = lds16(L, xrb + ((c) & 3) * 4096 + (((2 * s4 + hh) ^ xsw) * 16)); \
      wk[(c) & 1][s4] = ldsfrag_tr(L, L_W + 28672 + (4 * (c) + s4) * 1024 + trb); \
      wv[(c) & 1][s4] = ldsfrag_tr(L, L_W + 57344 + (4 * (c) + s4) * 1024 + trb); SEND \
    } while (0)
#define A_STEP(c, s4) do { const v4u xx = xf[4 * (c) + (s4)]; \
      ak = mfma32(xx, wk[(c) & 1][s4], ak); av = mfma32(xx, wv[(c) & 1][s4], av); \
      sq = mfma4(xx.x, xx.y, xx.x, xx.y, sq); sm = mfma4(ONES, ONES, xx.x, xx.y, sm); \
      sq = mfma4(xx.z, xx.w, xx.z, xx.w, sq); sm = mfma4(ONES, ONES, xx.z, xx.w, sm); } while (0)
  const int hw = ((idx - 28) * 8 + xcd) * 4 + w;
  const rsrc_t rsO_ = mkrs(mlp ? wo : w2, mlp ? D * D * 2 : D * F * 2), rs1 = mkrs(w1, D * F * 2);
  const int vrow = (lane >> 3) * 896 + (lane & 7) * 16;
  v4u rw[72];
#define PIECE(q) (hw + 128 * (q))
  int rbase[4];
#pragma unroll
  for (int q = 0; q < 4; q++) {
    const int pc = min(PIECE(q), 391), kc = (pc * 9363) >> 16;
    rbase[q] = mlp ? (28 * (4 * w + q) - 4 * (q & 1)) * 896 + j * 128 : 32 * kc * 896 + (pc - 7 * kc) * 128;
  }
  const int vlo = (mlp && lane < 32) ? OOB : vrow, vhi = (mlp && lane >= 32) ? OOB : vrow;
  const int v1 = mlp ? (lane >> 3) * 3584 + (lane & 7) * 16 + fa * 64 : OOB;
  const int r1 = ROT1;
#define W1_KS(k) ((k) + r1 >= 28 ? (k) + r1 - 28 : (k) + r1)
#define LD_RW(i) do { \
    if constexpr ((i) < 16) rw[i] = ld16<0>(rsO_, ((i) & 7) == 4 ? vlo : (((i) & 7) == 3 ? vhi : vrow), rbase[(i) >> 2] + ((i) & 3) * 8 * 896); \
    else rw[i] = ld16<0>(rs1, v1, (2 * W1_KS(((i) - 16) >> 1) + ((i) & 1)) * 8 * 3584); } while (0)
  unsigned tch = 0;
  waitv<12>(); BAR();
  STAMP(2);
  A_READS(0);
  SB();
  SFOR(c, 0, 7)
    ek = ldsfrag_tr(L, L_W + 28672 + (4 * c) * 1024 + w * 1024 + trb);
    ev = ldsfrag_tr(L, L_W + 57344 + (4 * c) * 1024 + w * 1024 + trb);
    A_STEP(c, 0);
    if (c < 4) { DMA_W(1, c + 3); DMA_W(2, c + 3); } else if (c < 6) DMA_W(0, 3 * (c - 4));
    if constexpr (c == 6) { SFOR(i, 0, NW0 / 3) LD_RW(i); SEND }
    SB();
    A_STEP(c, 1);
    if (c < 4) { DMA_X(c + 3, 0); DMA_X(c + 3, 1); } else if (c < 6) DMA_W(0, 3 * (c - 4) + 1);
    if constexpr (c == 6) { SFOR(i, NW0 / 3, 2 * NW0 / 3) LD_RW(i); SEND }
    SB();
    A_STEP(c, 2);
    if (c < 4) { DMA_X(c + 3, 2); DMA_X(c + 3, 3); } else if (c < 6) DMA_W(0, 3 * (c - 4) + 2);
    if constexpr (c == 6) { SFOR(i, 2 * NW0 / 3, NW0) LD_RW(i); SEND }
    if (TOUCH && c == 2) {
      const int t = (b * 4 + w) * 32 + r;
      const u16* tp = t < 3136 ? wo + t * 64 : (t < 15680 ? w1 + (t - 3136) * 64 : w2 + (min(t, 28223) - 15680) * 64);
      asm volatile("global_load_dword %0, %1, off" : "=v"(tch) : "v"(tp) : "memory");
    }
    ck = mfma4(ONES, ONES, ek.x, ek.y, ck); cv = mfma4(ONES, ONES, ev.x, ev.y, cv);
    ck = mfma4(ONES, ONES, ek.z, ek.w, ck); cv = mfma4(ONES, ONES, ev.z, ev.w, cv);
    SB();
    if constexpr (c < 6) {
      if (c < 2) waitv<12>(); else if (c < 4) waitv<12 + TOUCH>(); else if (c == 4) waitv<9 + TOUCH>(); else waitv<6>();
      BAR();
      A_READS(c + 1);
      SB();
    }
    A_STEP(c, 3);
    if (c == 5) DMA_W(0, 6);
    SB();
  SEND
  STAMP(4);
  float ssq = (lane & 2) ? ((lane & 1) ? sq[3] : sq[2]) : ((lane & 1) ? sq[1] : sq[0]);
  ssq = halfsum(ssq);
  const float mean = halfsum(sm[0]) * (1.f / D);
  const float var = fmaxf(ssq * (1.f / D) - mean * mean, 0.f);
  const float rs = __builtin_amdgcn_rsqf(var + 1e-5f), mrs = mean * rs;
  LAS float* stt = (LAS float*)(L + L_STT) + w * 64;
  LAS float* csx = (LAS float*)(L + L_CSX);
  {
    const float cks = halfsum(ck[0]), cvs = halfsum(cv[0]);
    stt[lane] = hh ? mrs : rs;
    csx[w * 64 + lane] = hh ? cvs : cks;
  }
  BAR();
  {
    const float csk = (csx[r] + csx[64 + r]) + (csx[128 + r] + csx[192 + r]);
    const float csv = (csx[32 + r] + csx[96 + r]) + (csx[160 + r] + csx[224 + r]);
#pragma unroll
    for (int q = 0; q < 4; q++) {
      const f32x4 r4 = *(LAS f32x4*)(stt + 8 * q + 4 * hh), m4 = *(LAS f32x4*)(stt + 32 + 8 * q + 4 * hh);
#pragma unroll
      for (int i = 0; i < 4; i++) {
        ak[4 * q + i] = phi6(fmaf(ak[4 * q + i], r4[i], fmaf(-m4[i], csk, 1e-6f)));
        av[4 * q + i] = fmaf(av[4 * q + i], r4[i], -m4[i] * csv);
      }
    }
    v4u p0 = packfrag<0>(ak), p1 = packfrag<1>(ak), v0 = packfrag<0>(av), v1 = packfrag<1>(av);
    if (r == (odd ? 0 : 28)) { v0 = v4u{ONES, ONES, ONES, ONES}; v1 = v0; }
    f32x16 kv = {};
    kv = mfma32(p0, v0, kv);
    kv = mfma32(p1, v1, kv);
    LAS char* red = L + L_X;
#pragma unroll
    for (int q = 0; q < 4; q++) *(LAS f32x4*)(red + (q * 4 + w) * 1024 + vo) = f32x4{kv[4 * q], kv[4 * q + 1], kv[4 * q + 2], kv[4 * q + 3]};
    waitv<NW0>();
    asm volatile("" :: "v"(tch), "v"(ctch));
    BAR();
    f32x4 t = *(LAS f32x4*)(red + (w * 4 + 0) * 1024 + vo) + *(LAS f32x4*)(red + (w * 4 + 1) * 1024 + vo);
    t += *(LAS f32x4*)(red + (w * 4 + 2) * 1024 + vo) + *(LAS f32x4*)(red + (w * 4 + 3) * 1024 + vo);
    v4u tu;
#pragma unroll
    for (int i = 0; i < 4; i++) tu[i] = (asu(t[i]) & ~0xFFu) | tag8;
    st16<SC1>(tu, rsS, vo, WS_KVP + ((head * 16 + tm) * 4 + w) * 1024);
    SB();
  }
  STAMP(5);
  LAS char* stg = L + L_TR + w * 6144;
  const int wro = (lane ^ ((lane >> 2) & 4)) * 16;
  const int rdo = ((lane >> 5) * 4 + ((lane & 15) >> 2)) * 128 + (((lane >> 3) & 1) * 64) + ((lane >> 4) & 1) * 32 + (lane & 3) * 8;
  v4u wo0[8], wo1[8];
  v4u wA[28], wB[28];
  f32x4 ca = {}, cb = {};
#define TR(n) do { \
    if constexpr ((n) < 8) transpose_unit(stg + ((n) % 3) * 2048, wro, rdo, rw[2 * (n)], rw[2 * (n) + 1], wo0[(n) & 7], wo1[(n) & 7]); \
    else { constexpr int k_ = (n) - 8; \
      transpose_unit(stg + ((n) % 3) * 2048, wro, rdo, rw[2 * (n)], rw[2 * (n) + 1], wA[k_], wB[k_]); \
      ca = mfma4(ONES, ONES, wA[k_].x, wA[k_].y, ca); cb = mfma4(ONES, ONES, wB[k_].x, wB[k_].y, cb); \
      ca = mfma4(ONES, ONES, wA[k_].z, wA[k_].w, ca); cb = mfma4(ONES, ONES, wB[k_].z, wB[k_].w, cb); } } while (0)
  constexpr int EQ = NW0 + NQ, E1 = EQ + NW1, E2 = E1 + NW2, E3 = E2 + NWB;
  constexpr int TQ = NW0 / 2, T1 = EQ / 2, T2 = E1 / 2, T3 = T2 + NT3, T4 = E2 / 2, T5 = E3 / 2;
  static_assert(TQ <= 7 && T1 >= 8 && T3 <= T4 && T4 - T3 <= 8 && E3 <= 72 && NWB <= 16 && NQ <= QP, "schedule");
  const int vow = mlp ? vo : OOB;
  bool trdone = false;
  {
    st16<SC1>(pois, rsS, vo, WS_OF + (atile * 32 + 2 * head) * 1024);
    st16<SC1>(pois, rsS, vo, WS_OF + (atile * 32 + 2 * head + 1) * 1024);
    st16<SC1>(pois, rsS, vow, WS_HF + (btile * 28 + 2 * ct) * 1024);
    st16<SC1>(pois, rsS, vow, WS_HF + (btile * 28 + 2 * ct + 1) * 1024);
    st4<SC1>(POISON, rsS, mlp ? lane * 4 : OOB, WS_HST + (btile * 14 + ct) * 256);
    SB();
    f32x16 aq = {};
    f32x4 cq = {}, cq2 = {}, t;
    v4u wq[4], p[16];
#define KV_LOADS() SFOR(i, 0, 16) p[i] = ld16<SC1>(rsS, vo, WS_KVP + ((head * 16 + i) * 4 + w) * 1024); SEND
#define KV_CHECK() unsigned bad = 0; t = f32x4{0.f, 0.f, 0.f, 0.f}; \
      SFOR(i, 0, 16) \
        const v4u y = p[i] ^ tag8;    \
        bad |= y.x | y.y; bad |= y.z | y.w; \
        t += __builtin_bit_cast(f32x4, y); SEND \
      const bool again = anyl((bad & 0xFFu) != 0)
    SFOR(s, 0, 4) wq[s] = ldsfrag_tr(L, L_W + s * 1024 + trb); SEND
    SFOR(s, 0, 28)
      const v4u wv_ = wq[s & 3];
      aq = mfma32(wv_, xf[s], aq);
      cq = mfma4(ONES, ONES, wv_.x, wv_.y, cq);
      cq2 = mfma4(ONES, ONES, wv_.z, wv_.w, cq2);
      if constexpr (s + 4 < 28) wq[s & 3] = ldsfrag_tr(L, L_W + (s + 4) * 1024 + trb);
      if constexpr (s >= QP && s < QP + 8) {
        p[2 * (s - QP)] = ld16<SC1>(rsS, vo, WS_KVP + ((head * 16 + 2 * (s - QP)) * 4 + w) * 1024);
        p[2 * (s - QP) + 1] = ld16<SC1>(rsS, vo, WS_KVP + ((head * 16 + 2 * (s - QP) + 1) * 4 + w) * 1024);
      }
      if constexpr (s < NQ) LD_RW(NW0 + s);
      if constexpr (s >= 2 && (s & 3) == 2 && (s - 2) / 4 < TQ) TR((s - 2) / 4);
      SB();
    SEND
    LAS float* cqx = (LAS float*)(L + L_CQX) + w * 64;
    cqx[lane] = halfsum(cq[0] + cq2[0]);
    SFOR(q, 0, 4)
      const f32x4 c4 = *(LAS f32x4*)(cqx + 8 * q + 4 * hh);
      SFOR(i, 0, 4) aq[4 * q + i] = phi6(fmaf(aq[4 * q + i], rs, fmaf(-mrs, c4[i], 1e-6f))); SEND
    SEND
    const v4u pq0 = packfrag<0>(aq), pq1 = packfrag<1>(aq);
    SB();
    waitv<16>();
    st4<SC1>(tag32, rsS, lane == 0 ? WS_FLAG2 + (atile >> 1) * 256 + ((atile & 1) * 16 + head) * 4 : (lane == 1 && mlp ? WS_FLAG2 + g * 256 + (32 + j * 4 + w) * 4 : OOB), 0);
    SFOR(i, EQ, E1) LD_RW(i); SEND
    SB();
    STAMP(6);
    {
      KV_CHECK();
      if (again) {
        KV_LOADS()
        SB();
        SFOR(n, TQ, T1) TR(n); SEND
        trdone = true;
        for (int spin = 0; spin < SPIN_CAP; spin++) {
          KV_CHECK();
          if (!again) break;
          MEMFENCE();
          KV_LOADS()
          SB();
        }
      }
    }
    STAMP(7);
    v2u o2 = {pk2(t[0], t[1]), pk2(t[2], t[3])};
    if (odd ? (w == 0 && hh == 0) : (w == 3 && hh == 1)) o2 = v2u{0, 0};
    *(LAS v2u*)(L + L_KVF + (w >> 1) * 1024 + vo + (w & 1) * 8) = o2;
    BAR();
    const v4u k0 = lds16(L, L_KVF + vo), k1 = lds16(L, L_KVF + 1024 + vo);
    f32x16 o = {};
    o = mfma32(k0, pq0, o);
    o = mfma32(k1, pq1, o);
    const unsigned tu = asu(odd ? o[0] : o[12]);
    auto sw = __builtin_amdgcn_permlane32_swap(tu, tu, false, false);
    const unsigned s0 = sw[0], s1 = sw[1];
    const float inv = __builtin_amdgcn_rcpf(asf(odd ? s0 : s1) + 1e-6f);
    SFOR(i, 0, 16) o[i] *= inv; SEND
    st16<SC1>(packfrag<0>(o), rsS, vo, WS_OF + (atile * 32 + 2 * head) * 1024);
    st16<SC1>(packfrag<1>(o), rsS, vo, WS_OF + (atile * 32 + 2 * head + 1) * 1024);
    SB();
  }
  STAMP(8);
  const int fo = (lane < 60 ? lane : 0) * 4;
  const unsigned f2 = ld4<SC1>(rsS, mlp ? fo : OOB, WS_FLAG2 + g * 256);
  SFOR(k, 0, 8) st16<SC1>(pois, rsS, vow, WS_H1F + ((2 * g + (k >> 2)) * 112 + 2 * fa + (k & 3)) * 1024); SEND
  SB();
  if (!trdone) { SFOR(n, TQ, T1) TR(n); SEND }
  SB();
  if (!mlp) {
    SFOR(u, 0, 8)
      const int pc = PIECE(u >> 1), kc = (pc * 9363) >> 16, cb_ = pc - 7 * kc;
      const int dst = pc < 392 ? WS_W2T + (2 * cb_ * 112 + 2 * kc + (u & 1)) * 1024 : OOB;
      st16<SC1>(wo0[u], rsS, vo, dst);
      st16<SC1>(wo1[u], rsS, vo, dst + 112 * 1024);
    SEND
    waitv<0>();
    st4<SC1>(tag32, rsS, lane == 0 ? WS_FLAG1 + hw * 4 : OOB, 0);
    STAMP_OUT(0, 31);
    return;
  }
  v4u u0[28], u1[28];
#define D_KS(i) (28 * w + ((i) < 14 ? 4 * ((i) >> 1) + ((i) & 1) : 4 * (((i) - 14) >> 1) + 2 + ((i) & 1)))
#define U_OFF(q) (WS_W2T + ((2 * j + ((q) & 1)) * 112 + D_KS((q) >> 1)) * 1024)
#define LD_U(q) do { if constexpr ((q) & 1) u1[(q) >> 1] = ld16<SC1>(rsS, vo, U_OFF(q)); else u0[(q) >> 1] = ld16<SC1>(rsS, vo, U_OFF(q)); } while (0)
  {
    v2u xr[4];
    SFOR(q, 0, 4) xr[q] = ld8<0>(rsX, (btile * 32 + r) * 896 + hh * 8, ct * 64 + q * 16); SEND
    SFOR(k, 0, (NW2 / 2 > T2 - T1 ? NW2 / 2 : T2 - T1))
      if constexpr (E1 + 2 * k < E2) { LD_RW(E1 + 2 * k); LD_RW(E1 + 2 * k + 1); }
      if constexpr (T1 + k < T2) TR(T1 + k);
      SB();
    SEND
    flag_wait(f2, rsS, fo, WS_FLAG2 + g * 256, tag32);
    STAMP(9);
    v4u o0[8], o1[8];
    f32x16 a00, a10, a01, a11;
#define O_LOADS() SFOR(i, 0, 8) \
        o0[i] = ld16<SC1>(rsS, vo, WS_OF + ((2 * g) * 32 + 8 * w + i) * 1024); \
        o1[i] = ld16<SC1>(rsS, vo, WS_OF + ((2 * g + 1) * 32 + 8 * w + i) * 1024); SEND
#define O_GEMM(FIRST) unsigned m = 0; a00 = f32x16{}; a10 = f32x16{}; a01 = f32x16{}; a11 = f32x16{}; \
      SFOR(i, 0, 8) \
        a00 = mfma32(wo0[i], o0[i], a00); a10 = mfma32(wo1[i], o0[i], a10); m = vmax(m, o0[i]); \
        a01 = mfma32(wo0[i], o1[i], a01); a11 = mfma32(wo1[i], o1[i], a11); m = vmax(m, o1[i]); \
        if constexpr (FIRST) { if constexpr (T3 + i < T4) TR(T3 + i); \
          if constexpr (E2 + 2 * i < E3) { LD_RW(E2 + 2 * i); LD_RW(E2 + 2 * i + 1); } } \
        SB(); SEND \
      const bool again = anyl(m == POISON)
    O_LOADS()
    SB();
    SFOR(n, T2, T3) TR(n); SEND
    SB();
    {
      O_GEMM(true);
      if (again) {
        for (int spin = 0; spin < SPIN_CAP; spin++) {
          MEMFENCE();
          O_LOADS()
          SB();
          O_GEMM(false);
          if (!again) break;
        }
      }
    }
    STAMP(10);
    const f32x16 a = reduce4(L, w, opaque(vo), a00, a10, a01, a11);
    unsigned hp[8];
    SFOR(q, 0, 4)
      hp[2 * q] = pk2(a[4 * q] + bflo(xr[q].x), a[4 * q + 1] + bfhi(xr[q].x));
      hp[2 * q + 1] = pk2(a[4 * q + 2] + bflo(xr[q].y), a[4 * q + 3] + bfhi(xr[q].y));
    SEND
    st16<SC1>(v4u{hp[0], hp[1], hp[2], hp[3]}, rsS, vo, WS_HF + (btile * 28 + 2 * ct) * 1024);
    st16<SC1>(v4u{hp[4], hp[5], hp[6], hp[7]}, rsS, vo, WS_HF + (btile * 28 + 2 * ct + 1) * 1024);
    float s1 = 0.f, s2 = 0.f;
    SFOR(q, 0, 8) const float lo = bflo(hp[q]), hi = bfhi(hp[q]); s1 += lo + hi; s2 = fmaf(lo, lo, fmaf(hi, hi, s2)); SEND
    s1 = halfsum(s1); s2 = halfsum(s2);
    st8<SC1>(v2u{asu(s1), asu(s2)}, rsS, hh == 0 ? r * 8 : OOB, WS_HST + (btile * 14 + ct) * 256);
    st4<SC1>(tag32, rsS, lane == 0 ? WS_FLAG3 + (g * 32 + j * 4 + w) * 4 : (lane == 1 && b == 0 && w == 0 ? WS_EPOCH : OOB), 0);
    *(LAS v4u*)(L + L_HP + w * 2048 + vo) = v4u{hp[0], hp[1], hp[2], hp[3]};
    *(LAS v4u*)(L + L_HP + w * 2048 + 1024 + vo) = v4u{hp[4], hp[5], hp[6], hp[7]};
    SB();
  }
  STAMP(11);
  constexpr int WIN = 14;
  v4u xw[WIN];
  const int hb0 = WS_H1F + (2 * g) * 112 * 1024;
#define H1_OFF(n) (hb0 + ((n) >= 28 ? 112 * 1024 : 0) + D_KS((n) % 28) * 1024)
  {
    const int f1o = (((14 * w + (lane < 14 ? lane : 0)) * 7 + j) & 127) * 4;
    const unsigned f1 = ld4<SC1>(rsS, f1o, WS_FLAG1);
    const float bv = bflo(b1[fa * 32 + lane]);
    BAR();
    SFOR(k, 0, (72 - E3) / 2)
      LD_RW(E3 + 2 * k); LD_RW(E3 + 2 * k + 1);
      if constexpr (T4 + k < T5) TR(T4 + k);
      SB();
    SEND
    flag_wait(f1, rsS, f1o, WS_FLAG1, tag32);
    SFOR(q, 0, NUP)
      dma16s(rsS, vo, U_OFF(q), lb + L_RED + (w * NUP + q) * 1024);
      if constexpr ((q & 1) && T4 + (72 - E3) / 2 + q / 2 < T5) TR(T4 + (72 - E3) / 2 + q / 2);
    SEND
    static_assert(T4 + (72 - E3) / 2 + NUP / 2 >= T5, "schedule");
    LAS float* c1x = (LAS float*)(L + L_C1) + w * 128;
    unsigned f3;
    {
      v4u hr[14]; v2u st[7];
#define H_LOADS() SFOR(i, 0, 14) hr[i] = ld16<SC1>(rsS, vo, WS_HF + ((2 * g) * 28 + 14 * w + i) * 1024); SEND \
        SFOR(i, 0, 7) st[i] = ld8<SC1>(rsS, lane * 8, WS_HST + ((2 * g + (w & 1)) * 14 + 2 * i) * 256); SEND
#define H_CHECK() unsigned m = 0; SFOR(i, 0, 14) m = vmax(m, hr[i]); SEND \
        SFOR(i, 0, 7) m = umax3(m, st[i].x, st[i].y); SEND const bool again = anyl(m == POISON)
      H_LOADS()
      SB();
      SFOR(n, T5, 36) TR(n); SEND
      {
        const float sa = halfsum(ca[0]), sb = halfsum(cb[0]);
        c1x[lane] = hh ? sb : sa; c1x[64 + lane] = bv;
      }
      SB();
      {
        H_CHECK();
        if (again) {
          for (int spin = 0; spin < SPIN_CAP; spin++) {
            MEMFENCE();
            H_LOADS()
            SB();
            H_CHECK();
            if (!again) break;
          }
        }
      }
      STAMP(12);
      f3 = ld4<SC1>(rsS, (r < 28 ? r : 0) * 4, WS_FLAG3 + g * 128);
      SFOR(i, 0, 14)
        const int ks = 14 * (w & 1) + i - r1;
        *(LAS v4u*)(L + L_H + ((w >> 1) * 28 + (ks < 0 ? ks + 28 : ks)) * 1024 + vo) = hr[i];
      SEND
      float s1 = 0.f, s2 = 0.f;
      SFOR(i, 0, 7) s1 += asf(st[i].x); s2 += asf(st[i].y); SEND
      s1 = halfsum(s1); s2 = halfsum(s2);
      const float mean2 = s1 * (1.f / D), var2 = fmaxf(s2 * (1.f / D) - mean2 * mean2, 0.f);
      const float rs2 = __builtin_amdgcn_rsqf(var2 + 1e-5f);
      if (w < 2 && hh == 0) *(LAS f32x2*)(L + L_ST2 + w * 256 + r * 8) = f32x2{rs2, -mean2 * rs2};
    }
    BAR();
    STAMP(13);
    const f32x2 sv0 = *(LAS f32x2*)(L + L_ST2 + r * 8), sv1 = *(LAS f32x2*)(L + L_ST2 + 256 + r * 8);
    constexpr int LA = 4;
    v4u hf[LA];
    SFOR(i, 0, LA) hf[i] = lds16(L, L_H + i * 1024 + vo); SEND
    f32x16 aA0 = {}, aB0 = {}, aA1 = {}, aB1 = {};
    constexpr float C1 = -2.f * 0.7978845608028654f * LOG2E, C2 = C1 * 0.044715f;
    SFOR(i, 0, 28)
      aA0 = mfma32(wA[i], hf[i % LA], aA0);
      aB0 = mfma32(wB[i], hf[i % LA], aB0);
      hf[i % LA] = lds16(L, L_H + (i + LA) * 1024 + vo);
      if constexpr (NUP + i < NUP + NUR) LD_U(NUP + i);
      SB();
    SEND
    STAMP(14);
    f32x4 cc[2], bb[2];
    unsigned pkd[4];
    float ya[2], eb[2];
#define CONSTS(grp) do { cc[(grp) & 1] = *(LAS f32x4*)(c1x + 8 * (grp) + 4 * hh); bb[(grp) & 1] = *(LAS f32x4*)(c1x + 64 + 8 * (grp) + 4 * hh); } while (0)
    CONSTS(0);
    SFOR(sl, 0, 56)
      constexpr int i = sl >> 1;
      if constexpr (sl & 1) {
        aB1 = mfma32(wB[i], hf[i % LA], aB1);
        if constexpr (i + LA < 28) hf[i % LA] = lds16(L, L_H + (28 + i + LA) * 1024 + vo);
        if constexpr (NUP + NUR + i < 56) LD_U(NUP + NUR + i);
      } else {
        aA1 = mfma32(wA[i], hf[i % LA], aA1);
      }
      constexpr int m7 = sl % 7, grp = sl / 7, pr = 2 * grp + (m7 >= 3 ? 1 : 0), stage = m7 >= 3 ? m7 - 3 : m7;
      if constexpr (m7 == 3 && grp < 7) CONSTS(grp + 1);
      if constexpr (m7 < 6) {
        SFOR(e, 0, 2)
          constexpr int reg = (2 * pr + e) & 15;
          if constexpr (stage == 0) {
            ya[e] = fmaf((grp < 4 ? aA0 : aB0)[reg], sv0.x, fmaf(sv0.y, cc[grp & 1][reg & 3], bb[grp & 1][reg & 3]));
            eb[e] = fmaf(ya[e] * ya[e], C2, C1);
          } else if constexpr (stage == 1) {
            eb[e] = 1.f + __builtin_amdgcn_exp2f(ya[e] * eb[e]);
          } else {
            ya[e] = ya[e] * __builtin_amdgcn_rcpf(eb[e]);
          }
        SEND
        if constexpr (stage == 2) pkd[pr & 3] = pk2(ya[0], ya[1]);
        else pin4(ya[0], ya[1], eb[0], eb[1]);
      }
      if constexpr (sl % 14 == 13) st16<SC1>(v4u{pkd[0], pkd[1], pkd[2], pkd[3]}, rsS, vo, hb0 + (2 * fa + sl / 14) * 1024);
      SB();
    SEND
    STAMP(15);
    flag_wait(f3, rsS, (r < 28 ? r : 0) * 4, WS_FLAG3 + g * 128, tag32);
    SFOR(n, 0, WIN) xw[n] = ld16<SC1>(rsS, vo, H1_OFF(n)); SEND
    SB();
    SFOR(fr, 0, 4)
      unsigned pq[4];
      SFOR(hq, 0, 2)
        constexpr int grp = 2 * fr + hq;
        const f32x4 c4 = *(LAS f32x4*)(c1x + 8 * grp + 4 * hh), b4 = *(LAS f32x4*)(c1x + 64 + 8 * grp + 4 * hh);
        float gv[4];
        SFOR(e, 0, 4) gv[e] = gelu_f(fmaf((grp < 4 ? aA1 : aB1)[(4 * grp + e) & 15], sv1.x, fmaf(sv1.y, c4[e], b4[e]))); SEND
        pq[2 * hq] = pk2(gv[0], gv[1]); pq[2 * hq + 1] = pk2(gv[2], gv[3]);
      SEND
      st16<SC1>(v4u{pq[0], pq[1], pq[2], pq[3]}, rsS, vo, hb0 + (112 + 2 * fa + fr) * 1024);
    SEND
    SB();
  }
  STAMP(16);
  {
    const rsrc_t rsB = mkrs(b2, D * 2), rsO = mkrs(out, S * D * 2);
    v2u br[4];
    SFOR(q, 0, 4) br[q] = ld8<0>(rsB, hh * 8, ct * 64 + q * 16); SEND
    const int vd = opaque(vo);
    SFOR(q, 0, NUP)
      if constexpr (q & 1) u1[q >> 1] = lds16(L, L_RED + (w * NUP + q) * 1024 + vd); else u0[q >> 1] = lds16(L, L_RED + (w * NUP + q) * 1024 + vd);
    SEND
    SB();
    f32x16 a00 = {}, a10 = {}, a01 = {}, a11 = {};
    SFOR(n, 0, 56)
      v4u y = xw[n % WIN];
      if (__builtin_expect(anyl(vmax(0u, y) == POISON), 0)) {
        for (int spin = 0; spin < SPIN_CAP; spin++) {
          MEMFENCE();
          y = ld16<SC1>(rsS, vo, H1_OFF(n));
          if (!anyl(vmax(0u, y) == POISON)) break;
          __builtin_amdgcn_s_sleep(1);
        }
      }
      if constexpr (n < 28) { a00 = mfma32(u0[n], y, a00); a10 = mfma32(u1[n], y, a10); }
      else { a01 = mfma32(u0[n - 28], y, a01); a11 = mfma32(u1[n - 28], y, a11); }
      if constexpr (n + WIN < 56) xw[n % WIN] = ld16<SC1>(rsS, vo, H1_OFF(n + WIN));
      SB();
      if constexpr (n == 0) STAMP(17);
      if constexpr (n == 27) STAMP(18);
    SEND
    STAMP(19);
    BAR();
    const f32x16 a = reduce4(L, w, vd, a00, a10, a01, a11);
    const v4u hq0 = lds16(L, L_HP + w * 2048 + vd), hq1 = lds16(L, L_HP + w * 2048 + 1024 + vd);
    const unsigned hp[8] = {hq0.x, hq0.y, hq0.z, hq0.w, hq1.x, hq1.y, hq1.z, hq1.w};
    SFOR(q, 0, 4)
      v2u o;
      o.x = pk2(a[4 * q] + bflo(br[q].x) + bflo(hp[2 * q]), a[4 * q + 1] + bfhi(br[q].x) + bfhi(hp[2 * q]));
      o.y = pk2(a[4 * q + 2] + bflo(br[q].y) + bflo(hp[2 * q + 1]), a[4 * q + 3] + bfhi(br[q].y) + bfhi(hp[2 * q + 1]));
      st8<0>(o, rsO, (btile * 32 + ((vd >> 4) & 31)) * 896 + (vd >> 9) * 8, ct * 64 + q * 16);
    SEND
  }
}

extern "C" void kernel_launch(void* const* d_in, const int* in_sizes, int n_in, void* d_out, int out_size, void* d_ws, size_t ws_size,
                              hipStream_t stream) {
  (void)hipFuncSetAttribute((const void*)fused, hipFuncAttributeMaxDynamicSharedMemorySize, L_BYTES);
  fused<<<256, 256, L_BYTES, stream>>>((const u16*)d_in[0], (const u16*)d_in[1], (const u16*)d_in[2], (const u16*)d_in[4], (const u16*)d_in[5],
                                       (const u16*)d_in[6], (const u16*)d_in[7], (u16*)d_out, (unsigned char*)d_ws);
}
```

```cpp
#include <hip/hip_runtime.h>
#include <hip/hip_bf16.h>
#include <stdint.h>

typedef unsigned short u16;
typedef unsigned v4u __attribute__((ext_vector_type(4)));
typedef unsigned v2u __attribute__((ext_vector_type(2)));
typedef float f32x2 __attribute__((ext_vector_type(2)));
typedef float f32x4 __attribute__((ext_vector_type(4)));
typedef float f32x16 __attribute__((ext_vector_type(16)));
typedef __bf16 bf16x8 __attribute__((ext_vector_type(8)));
typedef __bf16 bf16x4 __attribute__((ext_vector_type(4)));
typedef __bf16 bf16x2 __attribute__((ext_vector_type(2)));
typedef short s16x4 __attribute__((ext_vector_type(4)));
#define LAS __attribute__((address_space(3)))
#define DI __device__ __forceinline__
typedef __amdgpu_buffer_rsrc_t rsrc_t;

constexpr int S = 2048, D = 448, F = 1792;
constexpr int WS_EPOCH = 0;
constexpr int WS_FLAG1 = 4096;
constexpr int WS_FLAG2 = 8192;
constexpr int WS_FLAG3 = 16384;
constexpr int WS_KVP = 65536;
constexpr int WS_OF = WS_KVP + 16 * 16 * 4096;
constexpr int WS_HF = WS_OF + 64 * 32 * 1024;
constexpr int WS_HST = WS_HF + 64 * 28 * 1024;
constexpr int WS_H1F = WS_HST + 64 * 14 * 256;
constexpr int WS_W2T = WS_H1F + 64 * 112 * 1024;
constexpr int WS_C1P = WS_W2T + 14 * 112 * 1024;
constexpr int WS_END = WS_C1P + 4 * 1792 * 4;
constexpr int OOB = 0x7ff00000;
#define STAMP(i)
#define STAMP_DECL
#define STAMP_OUT(lo, hi)

template <int I> struct IC { static constexpr int v = I; constexpr operator int() const { return I; } };
template <int LO, int HI, class Fn> DI void sfor(Fn&& fn) {
  if constexpr (LO < HI) { fn(IC<LO>{}); sfor<LO + 1, HI>(fn); }
}
#define SFOR(i, lo, hi) sfor<(lo), (hi)>([&](auto i##_) __attribute__((always_inline)) { constexpr int i = decltype(i##_)::v;
#define SEND });

DI f32x16 mfma32(v4u a, v4u b, f32x16 c) {
  return __builtin_amdgcn_mfma_f32_32x32x16_bf16(__builtin_bit_cast(bf16x8, a), __builtin_bit_cast(bf16x8, b), c, 0, 0, 0);
}
DI f32x4 mfma4(unsigned a0, unsigned a1, unsigned b0, unsigned b1, f32x4 c) {
  v2u a = {a0, a1}, b = {b0, b1};
  return __builtin_amdgcn_mfma_f32_4x4x4bf16_1k(__builtin_bit_cast(s16x4, a), __builtin_bit_cast(s16x4, b), c, 0, 0, 0);
}
DI unsigned pk2(float lo, float hi) { bf16x2 v = {(__bf16)lo, (__bf16)hi}; return __builtin_bit_cast(unsigned, v); }
DI float asf(unsigned u) { return __builtin_bit_cast(float, u); }
DI unsigned asu(float f) { return __builtin_bit_cast(unsigned, f); }
DI float bflo(unsigned u) { return asf(u << 16); }
DI float bfhi(unsigned u) { return asf(u & 0xffff0000u); }
DI float halfsum(float a) {
  unsigned u = asu(a);
  auto r = __builtin_amdgcn_permlane32_swap(u, u, false, false);
  const unsigned r0 = r[0], r1 = r[1];
  return asf(r0) + asf(r1);
}
DI rsrc_t mkrs(const void* p, int bytes) { return __builtin_amdgcn_make_buffer_rsrc((void*)p, 0, bytes, 0x00020000); }
constexpr int SC1 = 16;
template <int AUX> DI v4u ld16(rsrc_t rs, int voff, int soff) { return __builtin_amdgcn_raw_buffer_load_b128(rs, voff, soff, AUX); }
template <int AUX> DI v2u ld8(rsrc_t rs, int voff, int soff) { return __builtin_amdgcn_raw_buffer_load_b64(rs, voff, soff, AUX); }
template <int AUX> DI unsigned ld4(rsrc_t rs, int voff, int soff) { return __builtin_amdgcn_raw_buffer_load_b32(rs, voff, soff, AUX); }
template <int AUX> DI void st16(v4u v, rsrc_t rs, int voff, int soff) { __builtin_amdgcn_raw_buffer_store_b128(v, rs, voff + soff, 0, AUX); }
template <int AUX> DI void st8(v2u v, rsrc_t rs, int voff, int soff) { __builtin_amdgcn_raw_buffer_store_b64(v, rs, voff, soff, AUX); }
template <int AUX> DI void st4(unsigned v, rsrc_t rs, int voff, int soff) { __builtin_amdgcn_raw_buffer_store_b32(v, rs, voff, soff, AUX); }
DI void dma16(rsrc_t rs, int voff, int soff, int lds) {
  unsigned keep;
  asm volatile("s_mov_b32 %0, m0\n\ts_mov_b32 m0, %1\n\ts_nop 0\n\tbuffer_load_dwordx4 %2, %3, %4 offen lds\n\ts_mov_b32 m0, %0"
               : "=&s"(keep) : "s"(lds), "v"(voff), "s"(rs), "s"(soff) : "memory");
}
DI void dma16s(rsrc_t rs, int voff, int soff, int lds) {
  unsigned keep;
  asm volatile("s_mov_b32 %0, m0\n\ts_mov_b32 m0, %1\n\ts_nop 0\n\tbuffer_load_dwordx4 %2, %3, %4 offen sc1 lds\n\ts_mov_b32 m0, %0"
               : "=&s"(keep) : "s"(lds), "v"(voff), "s"(rs), "s"(soff) : "memory");
}
DI void ld4_asm(unsigned& d, const void* p) { asm volatile("global_load_dword %0, %1, off" : "=v"(d) : "v"(p) : "memory"); }
template <int N> DI void waitv() { asm volatile("s_waitcnt vmcnt(%0)" ::"n"(N) : "memory"); }
#define BAR() asm volatile("s_waitcnt lgkmcnt(0)\n\ts_barrier" ::: "memory")
#define SB() __builtin_amdgcn_sched_barrier(0)
#define MEMFENCE() asm volatile("" ::: "memory")
DI v4u lds16(LAS char* p, int off) { return *(LAS v4u*)(p + off); }
DI v2u ldstr(LAS char* p, int off) { return __builtin_bit_cast(v2u, __builtin_amdgcn_ds_read_tr16_b64_v4bf16((LAS bf16x4*)(p + off))); }
DI v4u cat(v2u a, v2u b) { return v4u{a.x, a.y, b.x, b.y}; }
DI v4u ldsfrag_tr(LAS char* p, int off) { return cat(ldstr(p, off), ldstr(p, off + 256)); }
template <int SS> DI v4u packfrag(const f32x16& a) {
  return v4u{pk2(a[8 * SS], a[8 * SS + 1]), pk2(a[8 * SS + 2], a[8 * SS + 3]), pk2(a[8 * SS + 4], a[8 * SS + 5]), pk2(a[8 * SS + 6], a[8 * SS + 7])};
}
constexpr unsigned ONES = 0x3F803F80u, POISON = 0xFFFFFFFFu;
constexpr float LOG2E = 1.4426950408889634f;
constexpr int SPIN_CAP = 1 << 16;
#define ROT ((tm + 2 * head) % 7)
#define TOUCH 1
#define ROT1 (4 * j + (xcd & 3))
#define NW0 12
#define NW1 8
#define NW2 16
#define NQ 12
#define NWB 12
#define NT3 2
#define TC 2
#define LK 2
#define EP1 35
#define NUP 12
#define NUR 16
#define QP 14
DI float clamp01(float x) { return __builtin_amdgcn_fmed3f(x, 0.f, 1.f); }
DI float phi6(float tp) { return fmaxf(tp, 1e-6f) + clamp01(__builtin_amdgcn_exp2f(tp * LOG2E)); }
DI bool anyl(bool p) { return __builtin_amdgcn_ballot_w64(p) != 0; }
DI void pin4(float& a, float& b, float& c, float& d) { asm volatile("" : "+v"(a), "+v"(b), "+v"(c), "+v"(d)); }
DI int opaque(int v) { asm volatile("" : "+v"(v)); return v; }
DI unsigned umax3(unsigned a, unsigned b, unsigned c) { return max(max(a, b), c); }
DI unsigned vmax(unsigned m, v4u a) { return umax3(umax3(m, a.x, a.y), a.z, a.w); }
DI float gelu_f(float y) {
  constexpr float C1 = -2.f * 0.7978845608028654f * LOG2E, C2 = C1 * 0.044715f;
  const float e = __builtin_amdgcn_exp2f(y * fmaf(y * y, C2, C1));
  return y * __builtin_amdgcn_rcpf(1.f + e);
}

DI void flag_wait(unsigned f, rsrc_t rs, int voff, int base, unsigned tag) {
  if (__builtin_expect(anyl(f != tag), 0)) {
    for (int spin = 0; spin < SPIN_CAP; spin++) {
      __builtin_amdgcn_s_sleep(1);
      MEMFENCE();
      if (!anyl(ld4<SC1>(rs, voff, base) != tag)) break;
    }
  }
  MEMFENCE();
}

constexpr int L_W = 0, L_X = 86016, L_MISC = L_X + 65536;
constexpr int L_STT = L_MISC, L_CSX = L_MISC + 1024, L_CQX = L_MISC + 2048, L_KVF = L_MISC + 3072, L_ST2 = L_MISC + 5120, L_C1 = L_ST2 + 512;
constexpr int L_BYTES = L_C1 + 2048;
constexpr int L_RED = L_X, L_H = 0, L_HP = L_X + 49152, L_TR = 57344;

DI void transpose_unit(LAS char* stg, int wro, int rdo, v4u lo, v4u hi, v4u& f0, v4u& f1) {
  *(LAS v4u*)(stg + wro) = lo;
  *(LAS v4u*)(stg + 1024 + wro) = hi;
  f0 = cat(ldstr(stg, rdo), ldstr(stg, 1024 + rdo));
  f1 = cat(ldstr(stg, rdo ^ 64), ldstr(stg, 1024 + (rdo ^ 64)));
}

DI f32x16 reduce4(LAS char* L, int w, int vo, const f32x16& a00, const f32x16& a10, const f32x16& a01, const f32x16& a11) {
  f32x16 own;
#define PUT(T, A) \
  if (w == T) own = A; \
  else { const int sl = w - (w > T ? 1 : 0); _Pragma("unroll") for (int q = 0; q < 4; q++) \
    *(LAS f32x4*)(L + L_RED + ((T * 3 + sl) * 4 + q) * 1024 + vo) = f32x4{A[4 * q], A[4 * q + 1], A[4 * q + 2], A[4 * q + 3]}; }
  PUT(0, a00) PUT(1, a10) PUT(2, a01) PUT(3, a11)
#undef PUT
  BAR();
#pragma unroll
  for (int sl = 0; sl < 3; sl++)
#pragma unroll
    for (int q = 0; q < 4; q++) {
      const f32x4 v = *(LAS f32x4*)(L + L_RED + ((w * 3 + sl) * 4 + q) * 1024 + vo);
#pragma unroll
      for (int j = 0; j < 4; j++) own[4 * q + j] += v[j];
    }
  return own;
}

__global__ __launch_bounds__(256, 1) void fused(const u16* __restrict__ x, const u16* __restrict__ wqkv, const u16* __restrict__ wo,
                                                const u16* __restrict__ w1, const u16* __restrict__ b1, const u16* __restrict__ w2,
                                                const u16* __restrict__ b2, u16* __restrict__ out, unsigned char* __restrict__ ws) {
  extern __shared__ __attribute__((aligned(16))) char smem_[];
  LAS char* L = (LAS char*)smem_;
  const int lb = (int)(size_t)L;
  STAMP_DECL; STAMP(0);
  const int b = blockIdx.x, xcd = b & 7, idx = b >> 3, tm = 8 * (xcd >> 2) + (idx >> 2), head = 4 * (xcd & 3) + (idx & 3), odd = head & 1;
  const int lane = threadIdx.x & 63, w = __builtin_amdgcn_readfirstlane(threadIdx.x >> 6), hh = lane >> 5, r = lane & 31, vo = lane * 16;
  const rsrc_t rsX = mkrs(x, S * D * 2), rsW = mkrs(wqkv, D * 3 * D * 2), rsS = mkrs(ws, WS_END);

  const int row0 = tm * 128 + w * 32, atile = tm * 4 + w;
  const int xs = L_X + w * 16384;
  const int voffW = (lane >> 2) * 2688 + (lane & 3) * 16 + ((56 * head) & ~15);
  const int voffX0 = (row0 + (lane >> 3)) * 896 + (((lane & 7) ^ (lane >> 4)) * 16);
  const int voffX1 = (row0 + 8 + (lane >> 3)) * 896 + (((lane & 7) ^ (4 + (lane >> 4))) * 16);
  const int c0 = ROT;
#define CHUNK(i) ((c0 + (i)) >= 7 ? (c0 + (i)) - 7 : (c0 + (i)))
#define DMA_W(m, i) dma16(rsW, voffW, (m) * 896 + (4 * CHUNK(i) + w) * 43008, lb + L_W + (m) * 28672 + (4 * (i) + w) * 1024)
#define DMA_X(i, q) dma16(rsX, ((q) & 1) ? voffX1 : voffX0, ((q) >> 1) * 14336 + CHUNK(i) * 128, lb + xs + ((i) & 3) * 4096 + (q) * 1024)
#define DMA_C(i) do { DMA_W(1, i); DMA_W(2, i); DMA_X(i, 0); DMA_X(i, 1); DMA_X(i, 2); DMA_X(i, 3); } while (0)
  unsigned ctch;
  {
    unsigned long long pc; unsigned len;
    asm volatile("fused_code_begin_%=:\n\ts_getpc_b64 %0\n\ts_mov_b32 %1, .Lfunc_end0 - fused_code_begin_%=" : "=s"(pc), "=s"(len));
    const unsigned off = min((unsigned)(((idx * 4 + w) * 4 + (lane & 3)) * 128), len - 8u) & ~3u;
    asm volatile("global_load_dword %0, %1, %2" : "=v"(ctch) : "v"(off), "s"(pc) : "memory");
  }
  DMA_C(0); DMA_C(1);
  if constexpr (LK == 3) DMA_C(2);
  SB();
  STAMP(1);
  unsigned epoch;
  asm volatile("s_load_dword %0, %1, 0x0\n\ts_waitcnt lgkmcnt(0)" : "=s"(epoch) : "s"(ws) : "memory");
  const unsigned tag32 = epoch * 1664525u + 1013904223u, tag8 = (epoch + 1u) & 0xFFu;
  const v4u pois = {POISON, POISON, POISON, POISON};
  const bool mlp = idx < 28;
  const int g = 4 * xcd + idx / 7, j = idx % 7;
  const int btile = 2 * g + (w >> 1), ct = 2 * j + (w & 1);
  const int fa = 8 * j + 2 * w;
  const int trb = ((lane >> 5) * 8 + ((lane & 15) >> 2)) * 64 + ((lane >> 4) & 1) * 32 + (lane & 3) * 8;
  const int xrb = xs + r * 128, xsw = (r >> 1) & 7;
  f32x16 ak = {}, av = {};
  f32x4 sq = {}, sm = {}, ck = {}, cv = {};
  v4u xf[28], wk[2][4], wv[2][4], ek, ev;
#define A_READS(c) do { \
    SFOR(s4, 0, 4) \
      xf[4 * (c) + s4] = lds16(L, xrb + ((c) & 3) * 4096 + (((2 * s4 + hh) ^ xsw) * 16)); \
      wk[(c) & 1][s4] = ldsfrag_tr(L, L_W + 28672 + (4 * (c) + s4) * 1024 + trb); \
      wv[(c) & 1][s4] = ldsfrag_tr(L, L_W + 57344 + (4 * (c) + s4) * 1024 + trb); SEND \
    } while (0)
  const int hw = ((idx - 28) * 8 + xcd) * 4 + w;
  const rsrc_t rsO_ = mkrs(mlp ? wo : w2, mlp ? D * D * 2 : D * F * 2), rs1 = mkrs(w1, D * F * 2);
  const int vrow = (lane >> 3) * 896 + (lane & 7) * 16;
  v4u rw[72];
#define PIECE(q) (hw + 128 * (q))
  int rbase[4];
#pragma unroll
  for (int q = 0; q < 4; q++) {
    const int pc = min(PIECE(q), 391), kc = (pc * 9363) >> 16;
    rbase[q] = mlp ? (28 * (4 * w + q) - 4 * (q & 1)) * 896 + j * 128 : 32 * kc * 896 + (pc - 7 * kc) * 128;
  }
  const int vlo = (mlp && lane < 32) ? OOB : vrow, vhi = (mlp && lane >= 32) ? OOB : vrow;
  const int v1 = (mlp || hw < 112) ? (lane >> 3) * 3584 + (lane & 7) * 16 + (mlp ? fa * 64 : (hw >> 2) * 128) : OOB;
  const int r1 = mlp ? ROT1 : 7 * (hw & 3);
#define W1_KS(k) ((k) + r1 >= 28 ? (k) + r1 - 28 : (k) + r1)
#define LD_RW(i) do { \
    if constexpr ((i) < 16) rw[i] = ld16<0>(rsO_, ((i) & 7) == 4 ? vlo : (((i) & 7) == 3 ? vhi : vrow), rbase[(i) >> 2] + ((i) & 3) * 8 * 896); \
    else rw[i] = ld16<0>(rs1, v1, (2 * W1_KS(((i) - 16) >> 1) + ((i) & 1)) * 8 * 3584); } while (0)
  unsigned tch = 0;
  waitv<6 * (LK - 1)>(); BAR();
  STAMP(2);
  A_READS(0);
  SB();
  auto vm_op = [&](auto c_, auto k_) __attribute__((always_inline)) {
    constexpr int c = decltype(c_)::v, k = decltype(k_)::v;
    if constexpr (c + LK < 7) { if constexpr (k < 2) DMA_W(1 + k, c + LK); else DMA_X(c + LK, k - 2); }
    else if constexpr (c < 6) { constexpr int q = 6 * (c + LK - 7) + k; if constexpr (q < 7) DMA_W(0, q); }
    else { SFOR(i, k * NW0 / 6, (k + 1) * NW0 / 6) LD_RW(i); SEND }
  };
  SFOR(c, 0, 7)
    ek = ldsfrag_tr(L, L_W + 28672 + (4 * c) * 1024 + w * 1024 + trb);
    ev = ldsfrag_tr(L, L_W + 57344 + (4 * c) * 1024 + w * 1024 + trb);
    SFOR(s4, 0, 4)
      if constexpr (s4 == 3 && c < 6) {
        if constexpr (LK == 3) { if constexpr (c < 2) waitv<12>(); else if constexpr (c < 5) waitv<12 + TOUCH>(); else waitv<7>(); }
        else { if constexpr (c == TC || c == TC + 1) waitv<6 + TOUCH>(); else waitv<6>(); }
        BAR();
        A_READS(c + 1);
        SB();
      }
      const v4u xx = xf[4 * c + s4];
      sq = mfma4(xx.x, xx.y, xx.x, xx.y, sq); sm = mfma4(ONES, ONES, xx.x, xx.y, sm);
      sq = mfma4(xx.z, xx.w, xx.z, xx.w, sq); sm = mfma4(ONES, ONES, xx.z, xx.w, sm);
      if constexpr (s4 == 2) {
        ck = mfma4(ONES, ONES, ek.x, ek.y, ck); cv = mfma4(ONES, ONES, ev.x, ev.y, cv);
        ck = mfma4(ONES, ONES, ek.z, ek.w, ck); cv = mfma4(ONES, ONES, ev.z, ev.w, cv);
      }
      SB();
      ak = mfma32(xx, wk[c & 1][s4], ak);
      SB();
      if constexpr (s4 < 3) vm_op(IC<c>{}, IC<2 * s4>{});
      if constexpr (s4 == 3 && c == 5 && LK == 2) DMA_W(0, 6);
      SB();
      av = mfma32(xx, wv[c & 1][s4], av);
      SB();
      if constexpr (s4 < 3) vm_op(IC<c>{}, IC<2 * s4 + 1>{});
      if constexpr (TOUCH && c == TC && s4 == 2) {
        const int t = (b * 4 + w) * 16 + (lane & 15);
        ld4_asm(tch, t < 3136 ? wo + t * 64 : w1 + (min(t, 15679) - 3136) * 64);
      }
      SB();
    SEND
  SEND
  STAMP(4);
  float ssq = (lane & 2) ? ((lane & 1) ? sq[3] : sq[2]) : ((lane & 1) ? sq[1] : sq[0]);
  ssq = halfsum(ssq);
  const float mean = halfsum(sm[0]) * (1.f / D);
  const float var = fmaxf(ssq * (1.f / D) - mean * mean, 0.f);
  const float rs = __builtin_amdgcn_rsqf(var + 1e-5f), mrs = mean * rs;
  LAS float* stt = (LAS float*)(L + L_STT) + w * 64;
  LAS float* csx = (LAS float*)(L + L_CSX);
  {
    const float cks = halfsum(ck[0]), cvs = halfsum(cv[0]);
    stt[lane] = hh ? mrs : rs;
    csx[w * 64 + lane] = hh ? cvs : cks;
  }
  BAR();
  {
    const float csk = (csx[r] + csx[64 + r]) + (csx[128 + r] + csx[192 + r]);
    const float csv = (csx[32 + r] + csx[96 + r]) + (csx[160 + r] + csx[224 + r]);
#pragma unroll
    for (int q = 0; q < 4; q++) {
      const f32x4 r4 = *(LAS f32x4*)(stt + 8 * q + 4 * hh), m4 = *(LAS f32x4*)(stt + 32 + 8 * q + 4 * hh);
#pragma unroll
      for (int i = 0; i < 4; i++) {
        ak[4 * q + i] = phi6(fmaf(ak[4 * q + i], r4[i], fmaf(-m4[i], csk, 1e-6f)));
        av[4 * q + i] = fmaf(av[4 * q + i], r4[i], -m4[i] * csv);
      }
    }
    v4u p0 = packfrag<0>(ak), p1 = packfrag<1>(ak), v0 = packfrag<0>(av), v1 = packfrag<1>(av);
    if (r == (odd ? 0 : 28)) { v0 = v4u{ONES, ONES, ONES, ONES}; v1 = v0; }
    f32x16 kv = {};
    kv = mfma32(p0, v0, kv);
    kv = mfma32(p1, v1, kv);
    LAS char* red = L + L_X;
#pragma unroll
    for (int q = 0; q < 4; q++) *(LAS f32x4*)(red + (q * 4 + w) * 1024 + vo) = f32x4{kv[4 * q], kv[4 * q + 1], kv[4 * q + 2], kv[4 * q + 3]};
    waitv<NW0>();
    asm volatile("" :: "v"(tch), "v"(ctch));
    BAR();
    f32x4 t = *(LAS f32x4*)(red + (w * 4 + 0) * 1024 + vo) + *(LAS f32x4*)(red + (w * 4 + 1) * 1024 + vo);
    t += *(LAS f32x4*)(red + (w * 4 + 2) * 1024 + vo) + *(LAS f32x4*)(red + (w * 4 + 3) * 1024 + vo);
    v4u tu;
#pragma unroll
    for (int i = 0; i < 4; i++) tu[i] = (asu(t[i]) & ~0xFFu) | tag8;
    st16<SC1>(tu, rsS, vo, WS_KVP + ((head * 16 + tm) * 4 + w) * 1024);
    SB();
  }
  STAMP(5);
  LAS char* stg = L + L_TR + w * 6144;
  const int wro = (lane ^ ((lane >> 2) & 4)) * 16;
  const int rdo = ((lane >> 5) * 4 + ((lane & 15) >> 2)) * 128 + (((lane >> 3) & 1) * 64) + ((lane >> 4) & 1) * 32 + (lane & 3) * 8;
  v4u wo0[8], wo1[8];
  v4u wA[28], wB[28];
#define TR(n) do { \
    if constexpr ((n) < 8) transpose_unit(stg + ((n) % 3) * 2048, wro, rdo, rw[2 * (n)], rw[2 * (n) + 1], wo0[(n) & 7], wo1[(n) & 7]); \
    else transpose_unit(stg + ((n) % 3) * 2048, wro, rdo, rw[2 * (n)], rw[2 * (n) + 1], wA[((n) - 8) % 28], wB[((n) - 8) % 28]); } while (0)
  constexpr int EQ = NW0 + NQ, E1 = EQ + NW1, E2 = E1 + NW2, E3 = E2 + NWB;
  constexpr int TQ = NW0 / 2, T1 = EQ / 2, T2 = E1 / 2, T3 = T2 + NT3, T4 = E2 / 2, T5 = E3 / 2;
  static_assert(E1 >= 30 && T1 <= 15 && TQ <= 7 && T1 >= 8 && T3 <= T4 && T4 - T3 <= 8 && E3 <= 72 && NWB <= 16 && NQ <= QP, "schedule");
  const int vow = mlp ? vo : OOB;
  bool trdone = false;
  {
    st16<SC1>(pois, rsS, vo, WS_OF + (atile * 32 + 2 * head) * 1024);
    st16<SC1>(pois, rsS, vo, WS_OF + (atile * 32 + 2 * head + 1) * 1024);
    st16<SC1>(pois, rsS, vow, WS_HF + (btile * 28 + 2 * ct) * 1024);
    st16<SC1>(pois, rsS, vow, WS_HF + (btile * 28 + 2 * ct + 1) * 1024);
    st4<SC1>(POISON, rsS, mlp ? lane * 4 : OOB, WS_HST + (btile * 14 + ct) * 256);
    SB();
    f32x16 aq = {};
    f32x4 cq = {}, cq2 = {}, t;
    v4u wq[4], p[16], eq;
#define KV_LOADS() SFOR(i, 0, 16) p[i] = ld16<SC1>(rsS, vo, WS_KVP + ((head * 16 + i) * 4 + w) * 1024); SEND
#define KV_CHECK() unsigned bad = 0; t = f32x4{0.f, 0.f, 0.f, 0.f}; \
      SFOR(i, 0, 16) \
        const v4u y = p[i] ^ tag8;    \
        bad |= y.x | y.y; bad |= y.z | y.w; \
        t += __builtin_bit_cast(f32x4, y); SEND \
      const bool again = anyl((bad & 0xFFu) != 0)
    SFOR(s, 0, 4) wq[s] = ldsfrag_tr(L, L_W + s * 1024 + trb); SEND
    SFOR(s, 0, 28)
      const v4u wv_ = wq[s & 3];
      aq = mfma32(wv_, xf[s], aq);
      if constexpr ((s & 3) == 0) eq = ldsfrag_tr(L, L_W + s * 1024 + w * 1024 + trb);
      if constexpr ((s & 3) == 3) { cq = mfma4(ONES, ONES, eq.x, eq.y, cq); cq2 = mfma4(ONES, ONES, eq.z, eq.w, cq2); }
      if constexpr (s + 4 < 28) wq[s & 3] = ldsfrag_tr(L, L_W + (s + 4) * 1024 + trb);
      if constexpr (s >= QP && s < QP + 8) {
        p[2 * (s - QP)] = ld16<SC1>(rsS, vo, WS_KVP + ((head * 16 + 2 * (s - QP)) * 4 + w) * 1024);
        p[2 * (s - QP) + 1] = ld16<SC1>(rsS, vo, WS_KVP + ((head * 16 + 2 * (s - QP) + 1) * 4 + w) * 1024);
      }
      if constexpr (s < NQ) LD_RW(NW0 + s);
      if constexpr (s >= 2 && (s & 3) == 2 && (s - 2) / 4 < TQ) TR((s - 2) / 4);
      SB();
    SEND
    LAS float* cqx = (LAS float*)(L + L_CQX);
    {
      const float cs = halfsum(cq[0] + cq2[0]);
      if (hh == 0) cqx[w * 32 + r] = cs;
    }
    BAR();
    SFOR(q, 0, 4)
      const f32x4 c4 = (*(LAS f32x4*)(cqx + 8 * q + 4 * hh) + *(LAS f32x4*)(cqx + 32 + 8 * q + 4 * hh)) +
                       (*(LAS f32x4*)(cqx + 64 + 8 * q + 4 * hh) + *(LAS f32x4*)(cqx + 96 + 8 * q + 4 * hh));
      SFOR(i, 0, 4) aq[4 * q + i] = phi6(fmaf(aq[4 * q + i], rs, fmaf(-mrs, c4[i], 1e-6f))); SEND
    SEND
    const v4u pq0 = packfrag<0>(aq), pq1 = packfrag<1>(aq);
    SB();
    waitv<16>();
    st4<SC1>(tag32, rsS, lane == 0 ? WS_FLAG2 + (atile >> 1) * 256 + ((atile & 1) * 16 + head) * 4 : (lane == 1 && mlp ? WS_FLAG2 + g * 256 + (32 + j * 4 + w) * 4 : OOB), 0);
    SFOR(i, EQ, E1) LD_RW(i); SEND
    SB();
    STAMP(6);
    {
      KV_CHECK();
      if (again) {
        KV_LOADS()
        SB();
        SFOR(n, TQ, T1) TR(n); SEND
        trdone = true;
        for (int spin = 0; spin < SPIN_CAP; spin++) {
          KV_CHECK();
          if (!again) break;
          MEMFENCE();
          KV_LOADS()
          SB();
        }
      }
    }
    STAMP(7);
    v2u o2 = {pk2(t[0], t[1]), pk2(t[2], t[3])};
    if (odd ? (w == 0 && hh == 0) : (w == 3 && hh == 1)) o2 = v2u{0, 0};
    *(LAS v2u*)(L + L_KVF + (w >> 1) * 1024 + vo + (w & 1) * 8) = o2;
    BAR();
    const v4u k0 = lds16(L, L_KVF + vo), k1 = lds16(L, L_KVF + 1024 + vo);
    f32x16 o = {};
    o = mfma32(k0, pq0, o);
    o = mfma32(k1, pq1, o);
    const unsigned tu = asu(odd ? o[0] : o[12]);
    auto sw = __builtin_amdgcn_permlane32_swap(tu, tu, false, false);
    const unsigned s0 = sw[0], s1 = sw[1];
    const float inv = __builtin_amdgcn_rcpf(asf(odd ? s0 : s1) + 1e-6f);
    SFOR(i, 0, 16) o[i] *= inv; SEND
    st16<SC1>(packfrag<0>(o), rsS, vo, WS_OF + (atile * 32 + 2 * head) * 1024);
    st16<SC1>(packfrag<1>(o), rsS, vo, WS_OF + (atile * 32 + 2 * head + 1) * 1024);
    SB();
  }
  STAMP(8);
  const int fo = (lane < 60 ? lane : 0) * 4;
  const unsigned f2 = ld4<SC1>(rsS, mlp ? fo : OOB, WS_FLAG2 + g * 256);
  SB();
  if (!trdone) { SFOR(n, TQ, T1) TR(n); SEND }
  SB();
  if (!mlp) {
    SFOR(u, 0, 8)
      const int pc = PIECE(u >> 1), kc = (pc * 9363) >> 16, cb_ = pc - 7 * kc;
      const int dst = pc < 392 ? WS_W2T + (2 * cb_ * 112 + 2 * kc + (u & 1)) * 1024 : OOB;
      st16<SC1>(wo0[u], rsS, vo, dst);
      st16<SC1>(wo1[u], rsS, vo, dst + 112 * 1024);
    SEND
    SFOR(n, T1, 15) TR(n); SEND
    f32x4 ca = {}, cb = {};
    SFOR(k, 0, 7)
      ca = mfma4(ONES, ONES, wA[k].x, wA[k].y, ca); cb = mfma4(ONES, ONES, wB[k].x, wB[k].y, cb);
      ca = mfma4(ONES, ONES, wA[k].z, wA[k].w, ca); cb = mfma4(ONES, ONES, wB[k].z, wB[k].w, cb);
    SEND
    const float sa = halfsum(ca[0]), sb = halfsum(cb[0]);
    st4<SC1>(asu(hh ? sb : sa), rsS, hw < 112 ? lane * 4 : OOB, WS_C1P + (hw & 3) * (F * 4) + (hw >> 2) * 256);
    waitv<0>();
    st4<SC1>(tag32, rsS, lane == 0 ? WS_FLAG1 + hw * 4 : OOB, 0);
    STAMP_OUT(0, 31);
    return;
  }
  v4u u0[28], u1[28];
#define D_KS(i) (28 * w + ((i) < 14 ? 4 * ((i) >> 1) + ((i) & 1) : 4 * (((i) - 14) >> 1) + 2 + ((i) & 1)))
#define U_OFF(q) (WS_W2T + ((2 * j + ((q) & 1)) * 112 + D_KS((q) >> 1)) * 1024)
#define LD_U(q) do { if constexpr ((q) & 1) u1[(q) >> 1] = ld16<SC1>(rsS, vo, U_OFF(q)); else u0[(q) >> 1] = ld16<SC1>(rsS, vo, U_OFF(q)); } while (0)
  {
    v2u xr[4];
    SFOR(q, 0, 4) xr[q] = ld8<0>(rsX, (btile * 32 + r) * 896 + hh * 8, ct * 64 + q * 16); SEND
    SFOR(k, 0, (NW2 / 2 > T2 - T1 ? NW2 / 2 : T2 - T1))
      if constexpr (E1 + 2 * k < E2) { LD_RW(E1 + 2 * k); LD_RW(E1 + 2 * k + 1); }
      if constexpr (T1 + k < T2) TR(T1 + k);
      SB();
    SEND
    flag_wait(f2, rsS, fo, WS_FLAG2 + g * 256, tag32);
    STAMP(9);
    v4u o0[8], o1[8];
    f32x16 a00, a10, a01, a11;
#define O_LOADS() SFOR(i, 0, 8) \
        o0[i] = ld16<SC1>(rsS, vo, WS_OF + ((2 * g) * 32 + 8 * w + i) * 1024); \
        o1[i] = ld16<SC1>(rsS, vo, WS_OF + ((2 * g + 1) * 32 + 8 * w + i) * 1024); SEND
#define O_GEMM(FIRST) unsigned m = 0; a00 = f32x16{}; a10 = f32x16{}; a01 = f32x16{}; a11 = f32x16{}; \
      SFOR(i, 0, 8) \
        a00 = mfma32(wo0[i], o0[i], a00); a10 = mfma32(wo1[i], o0[i], a10); m = vmax(m, o0[i]); \
        a01 = mfma32(wo0[i], o1[i], a01); a11 = mfma32(wo1[i], o1[i], a11); m = vmax(m, o1[i]); \
        if constexpr (FIRST) { if constexpr (T3 + i < T4) TR(T3 + i); \
          if constexpr (E2 + 2 * i < E3) { LD_RW(E2 + 2 * i); LD_RW(E2 + 2 * i + 1); } } \
        SB(); SEND \
      const bool again = anyl(m == POISON)
    O_LOADS()
    SB();
    SFOR(n, T2, T3) TR(n); SEND
    SB();
    {
      O_GEMM(true);
      if (again) {
        for (int spin = 0; spin < SPIN_CAP; spin++) {
          MEMFENCE();
          O_LOADS()
          SB();
          O_GEMM(false);
          if (!again) break;
        }
      }
    }
    STAMP(10);
    const f32x16 a = reduce4(L, w, opaque(vo), a00, a10, a01, a11);
    unsigned hp[8];
    SFOR(q, 0, 4)
      hp[2 * q] = pk2(a[4 * q] + bflo(xr[q].x), a[4 * q + 1] + bfhi(xr[q].x));
      hp[2 * q + 1] = pk2(a[4 * q + 2] + bflo(xr[q].y), a[4 * q + 3] + bfhi(xr[q].y));
    SEND
    st16<SC1>(v4u{hp[0], hp[1], hp[2], hp[3]}, rsS, vo, WS_HF + (btile * 28 + 2 * ct) * 1024);
    st16<SC1>(v4u{hp[4], hp[5], hp[6], hp[7]}, rsS, vo, WS_HF + (btile * 28 + 2 * ct + 1) * 1024);
    float s1 = 0.f, s2 = 0.f;
    SFOR(q, 0, 8) const float lo = bflo(hp[q]), hi = bfhi(hp[q]); s1 += lo + hi; s2 = fmaf(lo, lo, fmaf(hi, hi, s2)); SEND
    s1 = halfsum(s1); s2 = halfsum(s2);
    st8<SC1>(v2u{asu(s1), asu(s2)}, rsS, hh == 0 ? r * 8 : OOB, WS_HST + (btile * 14 + ct) * 256);
    if (b == 0 && threadIdx.x == 0) st4<SC1>(tag32, rsS, 0, WS_EPOCH);
    *(LAS v4u*)(L + L_HP + w * 2048 + vo) = v4u{hp[0], hp[1], hp[2], hp[3]};
    *(LAS v4u*)(L + L_HP + w * 2048 + 1024 + vo) = v4u{hp[4], hp[5], hp[6], hp[7]};
    SB();
  }
  STAMP(11);
  constexpr int WIN = 14;
  v4u xw[WIN];
  const int hb0 = WS_H1F + (2 * g) * 112 * 1024;
#define H1_OFF(n) (hb0 + ((n) >= 28 ? 112 * 1024 : 0) + D_KS((n) % 28) * 1024)
  {
    const int f1o = (lane < 14 ? ((14 * w + lane) * 7 + j) & 127 : 2 * fa + (lane & 3)) * 4;
    const unsigned f1 = ld4<SC1>(rsS, f1o, WS_FLAG1);
    const float bv = bflo(b1[fa * 32 + lane]);
    BAR();
    SFOR(k, 0, (72 - E3) / 2)
      LD_RW(E3 + 2 * k); LD_RW(E3 + 2 * k + 1);
      if constexpr (T4 + k < T5) TR(T4 + k);
      SB();
    SEND
    flag_wait(f1, rsS, f1o, WS_FLAG1, tag32);
    unsigned cp[4];
    SFOR(q, 0, 4) cp[q] = ld4<SC1>(rsS, lane * 4, WS_C1P + q * (F * 4) + fa * 128); SEND
    SFOR(q, 0, NUP)
      dma16s(rsS, vo, U_OFF(q), lb + L_RED + (w * NUP + q) * 1024);
      if constexpr ((q & 1) && T4 + (72 - E3) / 2 + q / 2 < T5) TR(T4 + (72 - E3) / 2 + q / 2);
    SEND
    static_assert(T4 + (72 - E3) / 2 + NUP / 2 >= T5, "schedule");
    LAS float* c1x = (LAS float*)(L + L_C1) + w * 128;
    unsigned f3;
    {
      v4u hr[14]; v2u st[7];
#define H_LOADS() SFOR(i, 0, 14) hr[i] = ld16<SC1>(rsS, vo, WS_HF + ((2 * g) * 28 + 14 * w + i) * 1024); SEND \
        SFOR(i, 0, 7) st[i] = ld8<SC1>(rsS, lane * 8, WS_HST + ((2 * g + (w & 1)) * 14 + 2 * i) * 256); SEND
#define H_CHECK() unsigned m = 0; SFOR(i, 0, 14) m = vmax(m, hr[i]); SEND \
        SFOR(i, 0, 7) m = umax3(m, st[i].x, st[i].y); SEND const bool again = anyl(m == POISON)
      H_LOADS()
      SB();
      SFOR(n, T5, 36) TR(n); SEND
      c1x[lane] = (asf(cp[0]) + asf(cp[1])) + (asf(cp[2]) + asf(cp[3])); c1x[64 + lane] = bv;
      SB();
      {
        H_CHECK();
        if (again) {
          for (int spin = 0; spin < SPIN_CAP; spin++) {
            MEMFENCE();
            H_LOADS()
            SB();
            H_CHECK();
            if (!again) break;
          }
        }
      }
      STAMP(12);
      SFOR(i, 0, 14)
        const int ks = 14 * (w & 1) + i - r1;
        *(LAS v4u*)(L + L_H + ((w >> 1) * 28 + (ks < 0 ? ks + 28 : ks)) * 1024 + vo) = hr[i];
      SEND
      float s1 = 0.f, s2 = 0.f;
      SFOR(i, 0, 7) s1 += asf(st[i].x); s2 += asf(st[i].y); SEND
      s1 = halfsum(s1); s2 = halfsum(s2);
      const float mean2 = s1 * (1.f / D), var2 = fmaxf(s2 * (1.f / D) - mean2 * mean2, 0.f);
      const float rs2 = __builtin_amdgcn_rsqf(var2 + 1e-5f);
      if (w < 2 && hh == 0) *(LAS f32x2*)(L + L_ST2 + w * 256 + r * 8) = f32x2{rs2, -mean2 * rs2};
    }
    BAR();
    STAMP(13);
    const f32x2 sv0 = *(LAS f32x2*)(L + L_ST2 + r * 8), sv1 = *(LAS f32x2*)(L + L_ST2 + 256 + r * 8);
    constexpr int LA = 4;
    v4u hf[LA];
    SFOR(i, 0, LA) hf[i] = lds16(L, L_H + i * 1024 + vo); SEND
    f32x16 aA0 = {}, aB0 = {}, aA1 = {}, aB1 = {};
    constexpr float C1 = -2.f * 0.7978845608028654f * LOG2E, C2 = C1 * 0.044715f;
    SFOR(i, 0, 28)
      aA0 = mfma32(wA[i], hf[i % LA], aA0);
      if constexpr (i < 8) st16<SC1>(pois, rsS, vo, hb0 + ((i >> 2) * 112 + 2 * fa + (i & 3)) * 1024);
      aB0 = mfma32(wB[i], hf[i % LA], aB0);
      hf[i % LA] = lds16(L, L_H + (i + LA) * 1024 + vo);
      if constexpr (i >= 28 - NUR) LD_U(NUP + i - (28 - NUR));
      SB();
    SEND
    waitv<NUR>();
    st4<SC1>(tag32, rsS, lane == 0 ? WS_FLAG3 + (g * 32 + j * 4 + w) * 4 : OOB, 0);
    STAMP(14);
    f32x4 cc[2], bb[2];
    unsigned pkd[4];
    float ya[2], eb[2];
#define CONSTS(grp) do { cc[(grp) & 1] = *(LAS f32x4*)(c1x + 8 * (grp) + 4 * hh); bb[(grp) & 1] = *(LAS f32x4*)(c1x + 64 + 8 * (grp) + 4 * hh); } while (0)
#define GELU_SLOT(sl, AA, AB, SV, TILE) do { \
      constexpr int m7 = (sl) % 7, grp = (sl) / 7, pr = 2 * grp + (m7 >= 3 ? 1 : 0), stage = m7 >= 3 ? m7 - 3 : m7; \
      if constexpr (m7 == 3) CONSTS((grp + 1) & 7); \
      if constexpr (m7 < 6) { \
        SFOR(e, 0, 2) \
          constexpr int reg = (2 * pr + e) & 15; \
          if constexpr (stage == 0) { \
            ya[e] = fmaf((grp < 4 ? AA : AB)[reg], SV.x, fmaf(SV.y, cc[grp & 1][reg & 3], bb[grp & 1][reg & 3])); \
            eb[e] = fmaf(ya[e] * ya[e], C2, C1); \
          } else if constexpr (stage == 1) { \
            eb[e] = 1.f + __builtin_amdgcn_exp2f(ya[e] * eb[e]); \
          } else { \
            ya[e] = ya[e] * __builtin_amdgcn_rcpf(eb[e]); \
          } \
        SEND \
        if constexpr (stage == 2) pkd[pr & 3] = pk2(ya[0], ya[1]); \
        else pin4(ya[0], ya[1], eb[0], eb[1]); \
      } \
      if constexpr ((sl) % 14 == 13) st16<SC1>(v4u{pkd[0], pkd[1], pkd[2], pkd[3]}, rsS, vo, hb0 + ((TILE) * 112 + 2 * fa + (sl) / 14) * 1024); } while (0)
    CONSTS(0);
    SFOR(sl, 0, 56)
      constexpr int i = sl >> 1;
      if constexpr (sl & 1) {
        aB1 = mfma32(wB[i], hf[i % LA], aB1);
        if constexpr (i + LA < 28) hf[i % LA] = lds16(L, L_H + (28 + i + LA) * 1024 + vo);
        if constexpr (NUP + NUR + i < 56) LD_U(NUP + NUR + i);
      } else {
        aA1 = mfma32(wA[i], hf[i % LA], aA1);
      }
      if constexpr (sl == 40) f3 = ld4<SC1>(rsS, (r < 28 ? r : 0) * 4, WS_FLAG3 + g * 128);
      GELU_SLOT(sl, aA0, aB0, sv0, 0);
      SB();
    SEND
    STAMP(15);
    flag_wait(f3, rsS, (r < 28 ? r : 0) * 4, WS_FLAG3 + g * 128, tag32);
    SFOR(n, 0, WIN) xw[n] = ld16<SC1>(rsS, vo, H1_OFF(n)); SEND
    const rsrc_t rsB = mkrs(b2, D * 2), rsO = mkrs(out, S * D * 2);
    const int vd = opaque(vo);
    SFOR(q, 0, NUP)
      if constexpr (q & 1) u1[q >> 1] = lds16(L, L_RED + (w * NUP + q) * 1024 + vd); else u0[q >> 1] = lds16(L, L_RED + (w * NUP + q) * 1024 + vd);
    SEND
    SB();
    SFOR(sl, 0, EP1) GELU_SLOT(sl, aA1, aB1, sv1, 1); SEND
    SB();
    STAMP(16);
    f32x16 a00 = {}, a10 = {}, a01 = {}, a11 = {}, own;
    v2u br[4];
#define PUT(T, A) do { if (w == (T)) own = A; else { const int sl_ = w - (w > (T) ? 1 : 0); \
      SFOR(q, 0, 4) *(LAS f32x4*)(L + L_H + (((T) * 3 + sl_) * 4 + q) * 1024 + vd) = f32x4{A[4 * q], A[4 * q + 1], A[4 * q + 2], A[4 * q + 3]}; SEND } } while (0)
    SFOR(gq, 0, 28)
      constexpr int n = 2 * gq;
      v4u y0 = xw[n % WIN], y1 = xw[(n + 1) % WIN];
      if (__builtin_expect(anyl(vmax(vmax(0u, y0), y1) == POISON), 0)) {
        for (int spin = 0; spin < SPIN_CAP; spin++) {
          MEMFENCE();
          y0 = ld16<SC1>(rsS, vo, H1_OFF(n)); y1 = ld16<SC1>(rsS, vo, H1_OFF(n + 1));
          if (!anyl(vmax(vmax(0u, y0), y1) == POISON)) break;
        }
      }
      SFOR(k, 0, 2)
        const v4u y = k ? y1 : y0;
        if constexpr (n < 28) a00 = mfma32(u0[n + k], y, a00); else a01 = mfma32(u0[n + k - 28], y, a01);
        if constexpr (EP1 + 4 * gq + 2 * k < 56) GELU_SLOT(EP1 + 4 * gq + 2 * k, aA1, aB1, sv1, 1);
        if constexpr (n < 28) a10 = mfma32(u1[n + k], y, a10); else a11 = mfma32(u1[n + k - 28], y, a11);
        if constexpr (EP1 + 4 * gq + 2 * k + 1 < 56) GELU_SLOT(EP1 + 4 * gq + 2 * k + 1, aA1, aB1, sv1, 1);
        if constexpr (n + k + WIN < 56) xw[(n + k) % WIN] = ld16<SC1>(rsS, vo, H1_OFF(n + k + WIN));
        SB();
      SEND
      if constexpr (gq == 14) { BAR(); SFOR(q, 0, 4) br[q] = ld8<0>(rsB, hh * 8, ct * 64 + q * 16); SEND }
      if constexpr (gq == 16) PUT(0, a00);
      if constexpr (gq == 19) PUT(1, a10);
      if constexpr (gq == 0) STAMP(17);
      if constexpr (gq == 13) STAMP(18);
    SEND
    STAMP(19);
    PUT(2, a01); PUT(3, a11);
    const v4u hq0 = lds16(L, L_HP + w * 2048 + vd), hq1 = lds16(L, L_HP + w * 2048 + 1024 + vd);
    BAR();
    SFOR(sl, 0, 3)
      SFOR(q, 0, 4)
        const f32x4 v = *(LAS f32x4*)(L + L_H + ((w * 3 + sl) * 4 + q) * 1024 + vd);
        SFOR(e, 0, 4) own[4 * q + e] += v[e]; SEND
      SEND
    SEND
    const unsigned hp[8] = {hq0.x, hq0.y, hq0.z, hq0.w, hq1.x, hq1.y, hq1.z, hq1.w};
    SFOR(q, 0, 4)
      v2u o;
      o.x = pk2(own[4 * q] + bflo(br[q].x) + bflo(hp[2 * q]), own[4 * q + 1] + bfhi(br[q].x) + bfhi(hp[2 * q]));
      o.y = pk2(own[4 * q + 2] + bflo(br[q].y) + bflo(hp[2 * q + 1]), own[4 * q + 3] + bfhi(br[q].y) + bfhi(hp[2 * q + 1]));
      st8<0>(o, rsO, (btile * 32 + ((vd >> 4) & 31)) * 896 + (vd >> 9) * 8, ct * 64 + q * 16);
    SEND
  }
}

extern "C" void kernel_launch(void* const* d_in, const int* in_sizes, int n_in, void* d_out, int out_size, void* d_ws, size_t ws_size,
                              hipStream_t stream) {
  (void)hipFuncSetAttribute((const void*)fused, hipFuncAttributeMaxDynamicSharedMemorySize, L_BYTES);
  fused<<<256, 256, L_BYTES, stream>>>((const u16*)d_in[0], (const u16*)d_in[1], (const u16*)d_in[2], (const u16*)d_in[4], (const u16*)d_in[5],
                                       (const u16*)d_in[6], (const u16*)d_in[7], (u16*)d_out, (unsigned char*)d_ws);
}
```

```cpp
#include <hip/hip_runtime.h>
#include <hip/hip_bf16.h>
#include <stdint.h>

typedef unsigned short u16;
typedef unsigned v4u __attribute__((ext_vector_type(4)));
typedef unsigned v2u __attribute__((ext_vector_type(2)));
typedef float f32x2 __attribute__((ext_vector_type(2)));
typedef float f32x4 __attribute__((ext_vector_type(4)));
typedef float f32x16 __attribute__((ext_vector_type(16)));
typedef __bf16 bf16x8 __attribute__((ext_vector_type(8)));
typedef __bf16 bf16x4 __attribute__((ext_vector_type(4)));
typedef __bf16 bf16x2 __attribute__((ext_vector_type(2)));
typedef short s16x4 __attribute__((ext_vector_type(4)));
#define LAS __attribute__((address_space(3)))
#define DI __device__ __forceinline__
typedef __amdgpu_buffer_rsrc_t rsrc_t;

constexpr int S = 2048, D = 448, F = 1792;
constexpr int WS_EPOCH = 0;
constexpr int WS_FLAG1 = 4096;
constexpr int WS_FLAG2 = 8192;
constexpr int WS_FLAG3 = 16384;
constexpr int WS_KVP = 65536;
constexpr int WS_OF = WS_KVP + 16 * 16 * 4096;
constexpr int WS_HF = WS_OF + 64 * 32 * 1024;
constexpr int WS_HST = WS_HF + 64 * 28 * 1024;
constexpr int WS_H1F = WS_HST + 64 * 14 * 256;
constexpr int WS_W2T = WS_H1F + 64 * 112 * 1024;
constexpr int WS_C1P = WS_W2T + 14 * 112 * 1024;
constexpr int WS_END = WS_C1P + 4 * 1792 * 4;
constexpr int OOB = 0x7ff00000;
#define STAMP(i)
#define STAMP_DECL
#define STAMP_OUT(lo, hi)

template <int I> struct IC { static constexpr int v = I; constexpr operator int() const { return I; } };
template <int LO, int HI, class Fn> DI void sfor(Fn&& fn) {
  if constexpr (LO < HI) { fn(IC<LO>{}); sfor<LO + 1, HI>(fn); }
}
#define SFOR(i, lo, hi) sfor<(lo), (hi)>([&](auto i##_) __attribute__((always_inline)) { constexpr int i = decltype(i##_)::v;
#define SEND });

DI f32x16 mfma32(v4u a, v4u b, f32x16 c) {
  return __builtin_amdgcn_mfma_f32_32x32x16_bf16(__builtin_bit_cast(bf16x8, a), __builtin_bit_cast(bf16x8, b), c, 0, 0, 0);
}
DI f32x4 mfma4(unsigned a0, unsigned a1, unsigned b0, unsigned b1, f32x4 c) {
  v2u a = {a0, a1}, b = {b0, b1};
  return __builtin_amdgcn_mfma_f32_4x4x4bf16_1k(__builtin_bit_cast(s16x4, a), __builtin_bit_cast(s16x4, b), c, 0, 0, 0);
}
DI unsigned pk2(float lo, float hi) { bf16x2 v = {(__bf16)lo, (__bf16)hi}; return __builtin_bit_cast(unsigned, v); }
DI float asf(unsigned u) { return __builtin_bit_cast(float, u); }
DI unsigned asu(float f) { return __builtin_bit_cast(unsigned, f); }
DI float bflo(unsigned u) { return asf(u << 16); }
DI float bfhi(unsigned u) { return asf(u & 0xffff0000u); }
DI float halfsum(float a) {
  unsigned u = asu(a);
  auto r = __builtin_amdgcn_permlane32_swap(u, u, false, false);
  const unsigned r0 = r[0], r1 = r[1];
  return asf(r0) + asf(r1);
}
DI rsrc_t mkrs(const void* p, int bytes) { return __builtin_amdgcn_make_buffer_rsrc((void*)p, 0, bytes, 0x00020000); }
constexpr int SC1 = 16;
template <int AUX> DI v4u ld16(rsrc_t rs, int voff, int soff) { return __builtin_amdgcn_raw_buffer_load_b128(rs, voff, soff, AUX); }
template <int AUX> DI v2u ld8(rsrc_t rs, int voff, int soff) { return __builtin_amdgcn_raw_buffer_load_b64(rs, voff, soff, AUX); }
template <int AUX> DI unsigned ld4(rsrc_t rs, int voff, int soff) { return __builtin_amdgcn_raw_buffer_load_b32(rs, voff, soff, AUX); }
template <int AUX> DI void st16(v4u v, rsrc_t rs, int voff, int soff) { __builtin_amdgcn_raw_buffer_store_b128(v, rs, voff + soff, 0, AUX); }
template <int AUX> DI void st8(v2u v, rsrc_t rs, int voff, int soff) { __builtin_amdgcn_raw_buffer_store_b64(v, rs, voff, soff, AUX); }
template <int AUX> DI void st4(unsigned v, rsrc_t rs, int voff, int soff) { __builtin_amdgcn_raw_buffer_store_b32(v, rs, voff, soff, AUX); }
DI void dma16(rsrc_t rs, int voff, int soff, int lds) {
  unsigned keep;
  asm volatile("s_mov_b32 %0, m0\n\ts_mov_b32 m0, %1\n\ts_nop 0\n\tbuffer_load_dwordx4 %2, %3, %4 offen lds\n\ts_mov_b32 m0, %0"
               : "=&s"(keep) : "s"(lds), "v"(voff), "s"(rs), "s"(soff) : "memory");
}
DI void ld4_asm(unsigned& d, const void* p) { asm volatile("global_load_dword %0, %1, off" : "=v"(d) : "v"(p) : "memory"); }
template <int N> DI void waitv() { asm volatile("s_waitcnt vmcnt(%0)" ::"n"(N) : "memory"); }
#define BAR() asm volatile("s_waitcnt lgkmcnt(0)\n\ts_barrier" ::: "memory")
#define SB() __builtin_amdgcn_sched_barrier(0)
#define MEMFENCE() asm volatile("" ::: "memory")
DI v4u lds16(LAS char* p, int off) { return *(LAS v4u*)(p + off); }
DI v2u ldstr(LAS char* p, int off) { return __builtin_bit_cast(v2u, __builtin_amdgcn_ds_read_tr16_b64_v4bf16((LAS bf16x4*)(p + off))); }
DI v4u cat(v2u a, v2u b) { return v4u{a.x, a.y, b.x, b.y}; }
DI v4u ldsfrag_tr(LAS char* p, int off) { return cat(ldstr(p, off), ldstr(p, off + 256)); }
template <int SS> DI v4u packfrag(const f32x16& a) {
  return v4u{pk2(a[8 * SS], a[8 * SS + 1]), pk2(a[8 * SS + 2], a[8 * SS + 3]), pk2(a[8 * SS + 4], a[8 * SS + 5]), pk2(a[8 * SS + 6], a[8 * SS + 7])};
}
constexpr unsigned ONES = 0x3F803F80u, POISON = 0xFFFFFFFFu;
constexpr float LOG2E = 1.4426950408889634f;
constexpr int SPIN_CAP = 1 << 16;
#define ROT ((tm + 2 * head) % 7)
#define TOUCH 1
#define ROT1 (4 * j + (xcd & 3))
#define NW0 12
#define NW1 8
#define NW2 16
#define NQ 12
#define NWB 12
#define NT3 2
#define DWIN 12
#define TC 2
#define LK 2
#define EP1 35
#define QP 14
DI float clamp01(float x) { return __builtin_amdgcn_fmed3f(x, 0.f, 1.f); }
DI float phi6(float tp) { return fmaxf(tp, 1e-6f) + clamp01(__builtin_amdgcn_exp2f(tp * LOG2E)); }
DI bool anyl(bool p) { return __builtin_amdgcn_ballot_w64(p) != 0; }
DI void pin4(float& a, float& b, float& c, float& d) { asm volatile("" : "+v"(a), "+v"(b), "+v"(c), "+v"(d)); }
DI int opaque(int v) { asm volatile("" : "+v"(v)); return v; }
DI unsigned umax3(unsigned a, unsigned b, unsigned c) { return max(max(a, b), c); }
DI unsigned vmax(unsigned m, v4u a) { return umax3(umax3(m, a.x, a.y), a.z, a.w); }
DI float gelu_f(float y) {
  constexpr float C1 = -2.f * 0.7978845608028654f * LOG2E, C2 = C1 * 0.044715f;
  const float e = __builtin_amdgcn_exp2f(y * fmaf(y * y, C2, C1));
  return y * __builtin_amdgcn_rcpf(1.f + e);
}

DI void flag_wait(unsigned f, rsrc_t rs, int voff, int base, unsigned tag) {
  if (__builtin_expect(anyl(f != tag), 0)) {
    for (int spin = 0; spin < SPIN_CAP; spin++) {
      MEMFENCE();
      if (!anyl(ld4<SC1>(rs, voff, base) != tag)) break;
    }
  }
  MEMFENCE();
}

constexpr int L_W = 0, L_X = 86016, L_MISC = L_X + 65536;
constexpr int L_STT = L_MISC, L_CSX = L_MISC + 1024, L_CQX = L_MISC + 2048, L_KVF = L_MISC + 3072, L_ST2 = L_MISC + 5120, L_C1 = L_ST2 + 512;
constexpr int L_BYTES = L_C1 + 2048;
constexpr int L_RED = L_X, L_H = 0, L_HP = L_X + 49152, L_TR = 57344;

DI void transpose_unit(LAS char* stg, int wro, int rdo, v4u lo, v4u hi, v4u& f0, v4u& f1) {
  *(LAS v4u*)(stg + wro) = lo;
  *(LAS v4u*)(stg + 1024 + wro) = hi;
  f0 = cat(ldstr(stg, rdo), ldstr(stg, 1024 + rdo));
  f1 = cat(ldstr(stg, rdo ^ 64), ldstr(stg, 1024 + (rdo ^ 64)));
}

DI f32x16 reduce4(LAS char* L, int w, int vo, const f32x16& a00, const f32x16& a10, const f32x16& a01, const f32x16& a11) {
  f32x16 own;
#define PUT(T, A) \
  if (w == T) own = A; \
  else { const int sl = w - (w > T ? 1 : 0); _Pragma("unroll") for (int q = 0; q < 4; q++) \
    *(LAS f32x4*)(L + L_RED + ((T * 3 + sl) * 4 + q) * 1024 + vo) = f32x4{A[4 * q], A[4 * q + 1], A[4 * q + 2], A[4 * q + 3]}; }
  PUT(0, a00) PUT(1, a10) PUT(2, a01) PUT(3, a11)
#undef PUT
  BAR();
#pragma unroll
  for (int sl = 0; sl < 3; sl++)
#pragma unroll
    for (int q = 0; q < 4; q++) {
      const f32x4 v = *(LAS f32x4*)(L + L_RED + ((w * 3 + sl) * 4 + q) * 1024 + vo);
#pragma unroll
      for (int j = 0; j < 4; j++) own[4 * q + j] += v[j];
    }
  return own;
}

__global__ __launch_bounds__(256, 1) void fused(const u16* __restrict__ x, const u16* __restrict__ wqkv, const u16* __restrict__ wo,
                                                const u16* __restrict__ w1, const u16* __restrict__ b1, const u16* __restrict__ w2,
                                                const u16* __restrict__ b2, u16* __restrict__ out, unsigned char* __restrict__ ws) {
  extern __shared__ __attribute__((aligned(16))) char smem_[];
  LAS char* L = (LAS char*)smem_;
  const int lb = (int)(size_t)L;
  STAMP_DECL; STAMP(0);
  const int b = blockIdx.x, xcd = b & 7, idx = b >> 3, tm = 8 * (xcd >> 2) + (idx >> 2), head = 4 * (xcd & 3) + (idx & 3), odd = head & 1;
  const int lane = threadIdx.x & 63, w = __builtin_amdgcn_readfirstlane(threadIdx.x >> 6), hh = lane >> 5, r = lane & 31, vo = lane * 16;
  const rsrc_t rsX = mkrs(x, S * D * 2), rsW = mkrs(wqkv, D * 3 * D * 2), rsS = mkrs(ws, WS_END);

  const int row0 = tm * 128 + w * 32, atile = tm * 4 + w;
  const int xs = L_X + w * 16384;
  const int voffW = (lane >> 2) * 2688 + (lane & 3) * 16 + ((56 * head) & ~15);
  const int voffX0 = (row0 + (lane >> 3)) * 896 + (((lane & 7) ^ (lane >> 4)) * 16);
  const int voffX1 = (row0 + 8 + (lane >> 3)) * 896 + (((lane & 7) ^ (4 + (lane >> 4))) * 16);
  const int c0 = ROT;
#define CHUNK(i) ((c0 + (i)) >= 7 ? (c0 + (i)) - 7 : (c0 + (i)))
#define DMA_W(m, i) dma16(rsW, voffW, (m) * 896 + (4 * CHUNK(i) + w) * 43008, lb + L_W + (m) * 28672 + (4 * (i) + w) * 1024)
#define DMA_X(i, q) dma16(rsX, ((q) & 1) ? voffX1 : voffX0, ((q) >> 1) * 14336 + CHUNK(i) * 128, lb + xs + ((i) & 3) * 4096 + (q) * 1024)
#define DMA_C(i) do { DMA_W(1, i); DMA_W(2, i); DMA_X(i, 0); DMA_X(i, 1); DMA_X(i, 2); DMA_X(i, 3); } while (0)
  unsigned ctch;
  {
    unsigned long long pc; unsigned len;
    asm volatile("fused_code_begin_%=:\n\ts_getpc_b64 %0\n\ts_mov_b32 %1, fused_text_end - fused_code_begin_%=" : "=s"(pc), "=s"(len));
    const unsigned off = min((unsigned)(((idx * 4 + w) * 4 + (lane & 3)) * 128), len - 8u) & ~3u;
    asm volatile("global_load_dword %0, %1, %2" : "=v"(ctch) : "v"(off), "s"(pc) : "memory");
  }
  DMA_C(0); DMA_C(1);
  if constexpr (LK == 3) DMA_C(2);
  SB();
  STAMP(1);
  unsigned epoch;
  asm volatile("s_load_dword %0, %1, 0x0\n\ts_waitcnt lgkmcnt(0)" : "=s"(epoch) : "s"(ws) : "memory");
  const unsigned tag32 = epoch * 1664525u + 1013904223u, tag8 = (epoch + 1u) & 0xFFu;
  const v4u pois = {POISON, POISON, POISON, POISON};
  const bool mlp = idx < 28;
  const int g = 4 * xcd + idx / 7, j = idx % 7;
  const int btile = 2 * g + (w >> 1), ct = 2 * j + (w & 1);
  const int fa = 8 * j + 2 * w;
  const int trb = ((lane >> 5) * 8 + ((lane & 15) >> 2)) * 64 + ((lane >> 4) & 1) * 32 + (lane & 3) * 8;
  const int xrb = xs + r * 128, xsw = (r >> 1) & 7;
  f32x16 ak = {}, av = {};
  f32x4 sq = {}, sm = {}, ck = {}, cv = {};
  v4u xf[28], wk[2][4], wv[2][4], ek, ev;
#define A_READS(c) do { \
    SFOR(s4, 0, 4) \
      xf[4 * (c) + s4] = lds16(L, xrb + ((c) & 3) * 4096 + (((2 * s4 + hh) ^ xsw) * 16)); \
      wk[(c) & 1][s4] = ldsfrag_tr(L, L_W + 28672 + (4 * (c) + s4) * 1024 + trb); \
      wv[(c) & 1][s4] = ldsfrag_tr(L, L_W + 57344 + (4 * (c) + s4) * 1024 + trb); SEND \
    } while (0)
  const int hw = ((idx - 28) * 8 + xcd) * 4 + w;
  const rsrc_t rsO_ = mkrs(mlp ? wo : w2, mlp ? D * D * 2 : D * F * 2), rs1 = mkrs(w1, D * F * 2);
  const int vrow = (lane >> 3) * 896 + (lane & 7) * 16;
  v4u rw[72];
#define PIECE(q) (hw + 128 * (q))
  int rbase[4];
#pragma unroll
  for (int q = 0; q < 4; q++) {
    const int pc = min(PIECE(q), 391), kc = (pc * 9363) >> 16;
    rbase[q] = mlp ? (28 * (4 * w + q) - 4 * (q & 1)) * 896 + j * 128 : 32 * kc * 896 + (pc - 7 * kc) * 128;
  }
  const int vlo = (mlp && lane < 32) ? OOB : vrow, vhi = (mlp && lane >= 32) ? OOB : vrow;
  const int v1 = (mlp || hw < 112) ? (lane >> 3) * 3584 + (lane & 7) * 16 + (mlp ? fa * 64 : (hw >> 2) * 128) : OOB;
  const int r1 = mlp ? ROT1 : 7 * (hw & 3);
#define W1_KS(k) ((k) + r1 >= 28 ? (k) + r1 - 28 : (k) + r1)
#define LD_RW(i) do { \
    if constexpr ((i) < 16) rw[i] = ld16<0>(rsO_, ((i) & 7) == 4 ? vlo : (((i) & 7) == 3 ? vhi : vrow), rbase[(i) >> 2] + ((i) & 3) * 8 * 896); \
    else rw[i] = ld16<0>(rs1, v1, (2 * W1_KS(((i) - 16) >> 1) + ((i) & 1)) * 8 * 3584); } while (0)
  unsigned tch = 0;
  waitv<6 * (LK - 1)>(); BAR();
  STAMP(2);
  A_READS(0);
  SB();
  auto vm_op = [&](auto c_, auto k_) __attribute__((always_inline)) {
    constexpr int c = decltype(c_)::v, k = decltype(k_)::v;
    if constexpr (c + LK < 7) { if constexpr (k < 2) DMA_W(1 + k, c + LK); else DMA_X(c + LK, k - 2); }
    else if constexpr (c < 6) { constexpr int q = 6 * (c + LK - 7) + k; if constexpr (q < 7) DMA_W(0, q); }
    else { SFOR(i, k * NW0 / 6, (k + 1) * NW0 / 6) LD_RW(i); SEND }
  };
  SFOR(c, 0, 7)
    ek = ldsfrag_tr(L, L_W + 28672 + (4 * c) * 1024 + w * 1024 + trb);
    ev = ldsfrag_tr(L, L_W + 57344 + (4 * c) * 1024 + w * 1024 + trb);
    SFOR(s4, 0, 4)
      if constexpr (s4 == 3 && c < 6) {
        if constexpr (LK == 3) { if constexpr (c < 2) waitv<12>(); else if constexpr (c < 5) waitv<12 + TOUCH>(); else waitv<7>(); }
        else { if constexpr (c == TC || c == TC + 1) waitv<6 + TOUCH>(); else waitv<6>(); }
        BAR();
        A_READS(c + 1);
        SB();
      }
      const v4u xx = xf[4 * c + s4];
      sq = mfma4(xx.x, xx.y, xx.x, xx.y, sq); sm = mfma4(ONES, ONES, xx.x, xx.y, sm);
      sq = mfma4(xx.z, xx.w, xx.z, xx.w, sq); sm = mfma4(ONES, ONES, xx.z, xx.w, sm);
      if constexpr (s4 == 2) {
        ck = mfma4(ONES, ONES, ek.x, ek.y, ck); cv = mfma4(ONES, ONES, ev.x, ev.y, cv);
        ck = mfma4(ONES, ONES, ek.z, ek.w, ck); cv = mfma4(ONES, ONES, ev.z, ev.w, cv);
      }
      SB();
      ak = mfma32(xx, wk[c & 1][s4], ak);
      SB();
      if constexpr (s4 < 3) vm_op(IC<c>{}, IC<2 * s4>{});
      if constexpr (s4 == 3 && c == 5 && LK == 2) DMA_W(0, 6);
      SB();
      av = mfma32(xx, wv[c & 1][s4], av);
      SB();
      if constexpr (s4 < 3) vm_op(IC<c>{}, IC<2 * s4 + 1>{});
      if constexpr (TOUCH && c == TC && s4 == 2) {
        const int t = (b * 4 + w) * 16 + (lane & 15);
        ld4_asm(tch, (lane & 16) ? w2 + min(t, 12543) * 64 : (t < 3136 ? wo + t * 64 : w1 + (min(t, 15679) - 3136) * 64));
      }
      SB();
    SEND
  SEND
  STAMP(4);
  float ssq = (lane & 2) ? ((lane & 1) ? sq[3] : sq[2]) : ((lane & 1) ? sq[1] : sq[0]);
  ssq = halfsum(ssq);
  const float mean = halfsum(sm[0]) * (1.f / D);
  const float var = fmaxf(ssq * (1.f / D) - mean * mean, 0.f);
  const float rs = __builtin_amdgcn_rsqf(var + 1e-5f), mrs = mean * rs;
  LAS float* stt = (LAS float*)(L + L_STT) + w * 64;
  LAS float* csx = (LAS float*)(L + L_CSX);
  {
    const float cks = halfsum(ck[0]), cvs = halfsum(cv[0]);
    stt[lane] = hh ? mrs : rs;
    csx[w * 64 + lane] = hh ? cvs : cks;
  }
  BAR();
  {
    const float csk = (csx[r] + csx[64 + r]) + (csx[128 + r] + csx[192 + r]);
    const float csv = (csx[32 + r] + csx[96 + r]) + (csx[160 + r] + csx[224 + r]);
#pragma unroll
    for (int q = 0; q < 4; q++) {
      const f32x4 r4 = *(LAS f32x4*)(stt + 8 * q + 4 * hh), m4 = *(LAS f32x4*)(stt + 32 + 8 * q + 4 * hh);
#pragma unroll
      for (int i = 0; i < 4; i++) {
        ak[4 * q + i] = phi6(fmaf(ak[4 * q + i], r4[i], fmaf(-m4[i], csk, 1e-6f)));
        av[4 * q + i] = fmaf(av[4 * q + i], r4[i], -m4[i] * csv);
      }
    }
    v4u p0 = packfrag<0>(ak), p1 = packfrag<1>(ak), v0 = packfrag<0>(av), v1 = packfrag<1>(av);
    if (r == (odd ? 0 : 28)) { v0 = v4u{ONES, ONES, ONES, ONES}; v1 = v0; }
    f32x16 kv = {};
    kv = mfma32(p0, v0, kv);
    kv = mfma32(p1, v1, kv);
    LAS char* red = L + L_X;
#pragma unroll
    for (int q = 0; q < 4; q++) *(LAS f32x4*)(red + (q * 4 + w) * 1024 + vo) = f32x4{kv[4 * q], kv[4 * q + 1], kv[4 * q + 2], kv[4 * q + 3]};
    waitv<NW0>();
    asm volatile("" :: "v"(tch), "v"(ctch));
    BAR();
    f32x4 t = *(LAS f32x4*)(red + (w * 4 + 0) * 1024 + vo) + *(LAS f32x4*)(red + (w * 4 + 1) * 1024 + vo);
    t += *(LAS f32x4*)(red + (w * 4 + 2) * 1024 + vo) + *(LAS f32x4*)(red + (w * 4 + 3) * 1024 + vo);
    v4u tu;
#pragma unroll
    for (int i = 0; i < 4; i++) tu[i] = (asu(t[i]) & ~0xFFu) | tag8;
    st16<SC1>(tu, rsS, vo, WS_KVP + ((head * 16 + tm) * 4 + w) * 1024);
    SB();
  }
  STAMP(5);
  LAS char* stg = L + L_TR + w * 6144;
  const int wro = (lane ^ ((lane >> 2) & 4)) * 16;
  const int rdo = ((lane >> 5) * 4 + ((lane & 15) >> 2)) * 128 + (((lane >> 3) & 1) * 64) + ((lane >> 4) & 1) * 32 + (lane & 3) * 8;
  v4u wo0[8], wo1[8];
  v4u wA[28], wB[28];
#define TR(n) do { \
    if constexpr ((n) < 8) transpose_unit(stg + ((n) % 3) * 2048, wro, rdo, rw[2 * (n)], rw[2 * (n) + 1], wo0[(n) & 7], wo1[(n) & 7]); \
    else transpose_unit(stg + ((n) % 3) * 2048, wro, rdo, rw[2 * (n)], rw[2 * (n) + 1], wA[((n) - 8) % 28], wB[((n) - 8) % 28]); } while (0)
  constexpr int EQ = NW0 + NQ, E1 = EQ + NW1, E2 = E1 + NW2, E3 = E2 + NWB;
  constexpr int TQ = NW0 / 2, T1 = EQ / 2, T2 = E1 / 2, T3 = T2 + NT3, T4 = E2 / 2, T5 = E3 / 2;
  static_assert(E1 >= 30 && T1 <= 15 && TQ <= 7 && T1 >= 8 && T3 <= T4 && T4 - T3 <= 16 && E3 <= 72 && NWB <= 16 && NQ <= QP, "schedule");
  bool trdone = false;
  {
    st16<SC1>(pois, rsS, vo, WS_OF + (atile * 32 + 2 * head) * 1024);
    st16<SC1>(pois, rsS, vo, WS_OF + (atile * 32 + 2 * head + 1) * 1024);
    SB();
    f32x16 aq = {};
    f32x4 cq = {}, cq2 = {}, t;
    v4u wq[4], p[16], eq;
#define KV_LOADS() SFOR(i, 0, 16) p[i] = ld16<SC1>(rsS, vo, WS_KVP + ((head * 16 + i) * 4 + w) * 1024); SEND
#define KV_CHECK() unsigned bad = 0; t = f32x4{0.f, 0.f, 0.f, 0.f}; \
      SFOR(i, 0, 16) \
        const v4u y = p[i] ^ tag8;    \
        bad |= y.x | y.y; bad |= y.z | y.w; \
        t += __builtin_bit_cast(f32x4, y); SEND \
      const bool again = anyl((bad & 0xFFu) != 0)
    SFOR(s, 0, 4) wq[s] = ldsfrag_tr(L, L_W + s * 1024 + trb); SEND
    SFOR(s, 0, 28)
      const v4u wv_ = wq[s & 3];
      aq = mfma32(wv_, xf[s], aq);
      SB();
      if constexpr ((s & 3) == 0) eq = ldsfrag_tr(L, L_W + s * 1024 + w * 1024 + trb);
      if constexpr ((s & 3) == 3) { cq = mfma4(ONES, ONES, eq.x, eq.y, cq); cq2 = mfma4(ONES, ONES, eq.z, eq.w, cq2); }
      if constexpr (s + 4 < 28) wq[s & 3] = ldsfrag_tr(L, L_W + (s + 4) * 1024 + trb);
      if constexpr (s >= QP && s < QP + 8) {
        p[2 * (s - QP)] = ld16<SC1>(rsS, vo, WS_KVP + ((head * 16 + 2 * (s - QP)) * 4 + w) * 1024);
        p[2 * (s - QP) + 1] = ld16<SC1>(rsS, vo, WS_KVP + ((head * 16 + 2 * (s - QP) + 1) * 4 + w) * 1024);
      }
      if constexpr (s < NQ) LD_RW(NW0 + s);
      if constexpr (s >= 2 && (s & 3) == 2 && (s - 2) / 4 < TQ) TR((s - 2) / 4);
      SB();
    SEND
    LAS float* cqx = (LAS float*)(L + L_CQX);
    {
      const float cs = halfsum(cq[0] + cq2[0]);
      if (hh == 0) cqx[w * 32 + r] = cs;
    }
    BAR();
    SFOR(q, 0, 4)
      const f32x4 c4 = (*(LAS f32x4*)(cqx + 8 * q + 4 * hh) + *(LAS f32x4*)(cqx + 32 + 8 * q + 4 * hh)) +
                       (*(LAS f32x4*)(cqx + 64 + 8 * q + 4 * hh) + *(LAS f32x4*)(cqx + 96 + 8 * q + 4 * hh));
      SFOR(i, 0, 4) aq[4 * q + i] = phi6(fmaf(aq[4 * q + i], rs, fmaf(-mrs, c4[i], 1e-6f))); SEND
    SEND
    const v4u pq0 = packfrag<0>(aq), pq1 = packfrag<1>(aq);
    SB();
    waitv<16>();
    st4<SC1>(tag32, rsS, lane == 0 ? WS_FLAG2 + (atile >> 1) * 256 + ((atile & 1) * 16 + head) * 4 : OOB, 0);
    SFOR(i, EQ, E1) LD_RW(i); SEND
    SB();
    STAMP(6);
    {
      KV_CHECK();
      if (again) {
        KV_LOADS()
        SB();
        SFOR(n, TQ, T1) TR(n); SEND
        trdone = true;
        for (int spin = 0; spin < SPIN_CAP; spin++) {
          KV_CHECK();
          if (!again) break;
          MEMFENCE();
          KV_LOADS()
          SB();
        }
      }
    }
    STAMP(7);
    v2u o2 = {pk2(t[0], t[1]), pk2(t[2], t[3])};
    if (odd ? (w == 0 && hh == 0) : (w == 3 && hh == 1)) o2 = v2u{0, 0};
    *(LAS v2u*)(L + L_KVF + (w >> 1) * 1024 + vo + (w & 1) * 8) = o2;
    BAR();
    const v4u k0 = lds16(L, L_KVF + vo), k1 = lds16(L, L_KVF + 1024 + vo);
    f32x16 o = {};
    o = mfma32(k0, pq0, o);
    o = mfma32(k1, pq1, o);
    const unsigned tu = asu(odd ? o[0] : o[12]);
    auto sw = __builtin_amdgcn_permlane32_swap(tu, tu, false, false);
    const unsigned s0 = sw[0], s1 = sw[1];
    const float inv = __builtin_amdgcn_rcpf(asf(odd ? s0 : s1) + 1e-6f);
    SFOR(i, 0, 16) o[i] *= inv; SEND
    st16<SC1>(packfrag<0>(o), rsS, vo, WS_OF + (atile * 32 + 2 * head) * 1024);
    st16<SC1>(packfrag<1>(o), rsS, vo, WS_OF + (atile * 32 + 2 * head + 1) * 1024);
    SB();
  }
  STAMP(8);
  const int fo = r * 4;
  const unsigned f2 = ld4<SC1>(rsS, mlp ? fo : OOB, WS_FLAG2 + g * 256);
  SB();
  if (!trdone) { SFOR(n, TQ, T1) TR(n); SEND }
  SB();
  if (!mlp) {
    SFOR(k, 0, 14) st16<SC1>(pois, rsS, vo, WS_HF + (hw * 14 + k) * 1024); SEND
    st16<SC1>(pois, rsS, vo, WS_HST + hw * 1792);
    st16<SC1>(pois, rsS, lane < 48 ? vo : OOB, WS_HST + hw * 1792 + 1024);
    SFOR(u, 0, 8)
      const int pc = PIECE(u >> 1), kc = (pc * 9363) >> 16, cb_ = pc - 7 * kc;
      const int dst = pc < 392 ? WS_W2T + (2 * cb_ * 112 + 2 * kc + (u & 1)) * 1024 : OOB;
      st16<SC1>(wo0[u], rsS, vo, dst);
      st16<SC1>(wo1[u], rsS, vo, dst + 112 * 1024);
      SB();
      if constexpr (u == 2) {
        waitv<6>();
        st4<SC1>(tag32, rsS, lane == 0 ? WS_FLAG2 + (hw >> 2) * 256 + (32 + (hw & 3)) * 4 : OOB, 0);
        SB();
      }
    SEND
    SFOR(n, T1, 15) TR(n); SEND
    f32x4 ca = {}, cb = {};
    SFOR(k, 0, 7)
      ca = mfma4(ONES, ONES, wA[k].x, wA[k].y, ca); cb = mfma4(ONES, ONES, wB[k].x, wB[k].y, cb);
      ca = mfma4(ONES, ONES, wA[k].z, wA[k].w, ca); cb = mfma4(ONES, ONES, wB[k].z, wB[k].w, cb);
    SEND
    const float sa = halfsum(ca[0]), sb = halfsum(cb[0]);
    st4<SC1>(asu(hh ? sb : sa), rsS, hw < 112 ? lane * 4 : OOB, WS_C1P + (hw & 3) * (F * 4) + (hw >> 2) * 256);
    SB();
    const int p1 = WS_H1F + ((hw >> 2) * 224 + (hw & 3) * 28) * 1024;
    SFOR(k, 0, 56)
      st16<SC1>(pois, rsS, vo, p1 + (k < 28 ? k : 84 + k) * 1024);
      SB();
      if constexpr (k == 9) { waitv<10>(); st4<SC1>(tag32, rsS, lane == 0 ? WS_FLAG1 + hw * 4 : OOB, 0); SB(); }
      if constexpr (k == 37) { waitv<10>(); st4<SC1>(tag32, rsS, lane == 0 ? WS_FLAG3 + (hw >> 2) * 128 + (hw & 3) * 4 : OOB, 0); SB(); }
    SEND
    waitv<0>();
    st4<SC1>(tag32, rsS, lane == 0 ? WS_FLAG3 + (hw >> 2) * 128 + (4 + (hw & 3)) * 4 : OOB, 0);
    STAMP_OUT(0, 31);
    return;
  }
  v4u u0[28], u1[28];
  int mo[6];
  SFOR(k, 0, 6) mo[k] = 16 * (j + 1 + k >= 7 ? j + 1 + k - 7 : j + 1 + k) + 4 * w; SEND
#define D_KS(i) ((i) < 4 ? 2 * fa + (i) : mo[(((i) + 8) >> 1) % 6] + ((i) < 16 ? 0 : 2) + ((i) & 1))
#define U_OFF(q) (WS_W2T + ((2 * j + ((q) & 1)) * 112 + D_KS((q) >> 1)) * 1024)
#define LD_U(q) do { if constexpr ((q) & 1) u1[(q) >> 1] = ld16<SC1>(rsS, vo, U_OFF(q)); else u0[(q) >> 1] = ld16<SC1>(rsS, vo, U_OFF(q)); } while (0)
  {
    const int rowo = (btile * 32 + (lane >> 2)) * 896 + ct * 64 + (lane & 3) * 16;
    const int tw = L_HP + w * 2048 + (lane >> 2) * 64 + (((lane & 3) ^ (lane >> 4)) * 16);
    const int tr_ = L_HP + w * 2048 + r * 64 + hh * 8, tx = (r >> 2) & 3;
    const v4u xa = ld16<0>(rsX, rowo, 0), xb = ld16<0>(rsX, rowo, 16 * 896);
    SFOR(k, 0, (NW2 / 2 > T2 - T1 ? NW2 / 2 : T2 - T1))
      if constexpr (E1 + 2 * k < E2) { LD_RW(E1 + 2 * k); LD_RW(E1 + 2 * k + 1); }
      if constexpr (T1 + k < T2) TR(T1 + k);
      SB();
    SEND
    flag_wait(f2, rsS, fo, WS_FLAG2 + g * 256, tag32);
    STAMP(9);
    v4u o0[8], o1[8];
    f32x16 a00, a10, a01, a11;
    const int vb = opaque(vo);
#define PUT(BASE, V, T, A) do { if (w == (T)) own = A; else { const int sl_ = w - (w > (T) ? 1 : 0); \
      SFOR(q, 0, 4) *(LAS f32x4*)(L + (BASE) + (((T) * 3 + sl_) * 4 + q) * 1024 + V) = f32x4{A[4 * q], A[4 * q + 1], A[4 * q + 2], A[4 * q + 3]}; SEND } } while (0)
#define GET(BASE, V) SFOR(sl, 0, 3) SFOR(q, 0, 4) \
        const f32x4 v = *(LAS f32x4*)(L + (BASE) + ((w * 3 + sl) * 4 + q) * 1024 + V); \
        SFOR(e, 0, 4) own[4 * q + e] += v[e]; SEND SEND SEND
    f32x16 own;
#define O_LOADS() SFOR(i, 0, 8) o0[i] = ld16<SC1>(rsS, vo, WS_OF + ((2 * g) * 32 + 8 * w + i) * 1024); SEND \
      SFOR(i, 0, 8) o1[i] = ld16<SC1>(rsS, vo, WS_OF + ((2 * g + 1) * 32 + 8 * w + i) * 1024); SEND
#define O_GEMM(FIRST) unsigned m = 0; a00 = f32x16{}; a10 = f32x16{}; a01 = f32x16{}; a11 = f32x16{}; \
      SFOR(i, 0, 16) \
        if constexpr (i < 8) a00 = mfma32(wo0[i], o0[i], a00); else a01 = mfma32(wo0[i - 8], o1[i - 8], a01); \
        SB(); \
        if constexpr (FIRST && E2 + i < E3) LD_RW(E2 + i); \
        if constexpr (FIRST && i == 13) f2h = ld4<SC1>(rsS, (lane & 3) * 4, WS_FLAG2 + g * 256 + 128); \
        SB(); \
        if constexpr (i < 8) a10 = mfma32(wo1[i], o0[i], a10); else a11 = mfma32(wo1[i - 8], o1[i - 8], a11); \
        SB(); \
        if constexpr (i < 8) m = vmax(m, o0[i]); else m = vmax(m, o1[i - 8]); \
        if constexpr (FIRST && T3 + i < T4) TR(T3 + i); \
        if constexpr (i == 10) PUT(L_RED, vb, 0, a00); \
        if constexpr (i == 13) PUT(L_RED, vb, 1, a10); \
        SB(); SEND \
      const bool again = anyl(m == POISON)
    O_LOADS()
    unsigned f2h;
    SB();
    *(LAS v4u*)(L + tw) = xa; *(LAS v4u*)(L + tw + 1024) = xb;
    v2u xr[4];
    SFOR(q, 0, 4) xr[q] = *(LAS v2u*)(L + tr_ + ((q ^ tx) * 16)); SEND
    SFOR(n, T2, T3) TR(n); SEND
    SB();
    {
      O_GEMM(true);
      if (again) {
        for (int spin = 0; spin < SPIN_CAP; spin++) {
          MEMFENCE();
          O_LOADS()
          SB();
          O_GEMM(false);
          if (!again) break;
        }
      }
    }
    STAMP(10);
    PUT(L_RED, vb, 2, a01); PUT(L_RED, vb, 3, a11);
    flag_wait(f2h, rsS, (lane & 3) * 4, WS_FLAG2 + g * 256 + 128, tag32);
    BAR();
    GET(L_RED, vb)
    const f32x16& a = own;
    unsigned hp[8];
    SFOR(q, 0, 4)
      hp[2 * q] = pk2(a[4 * q] + bflo(xr[q].x), a[4 * q + 1] + bfhi(xr[q].x));
      hp[2 * q + 1] = pk2(a[4 * q + 2] + bflo(xr[q].y), a[4 * q + 3] + bfhi(xr[q].y));
    SEND
    st16<SC1>(v4u{hp[0], hp[1], hp[2], hp[3]}, rsS, vo, WS_HF + (btile * 28 + 2 * ct) * 1024);
    st16<SC1>(v4u{hp[4], hp[5], hp[6], hp[7]}, rsS, vo, WS_HF + (btile * 28 + 2 * ct + 1) * 1024);
    float s1 = 0.f, s2 = 0.f;
    SFOR(q, 0, 8) const float lo = bflo(hp[q]), hi = bfhi(hp[q]); s1 += lo + hi; s2 = fmaf(lo, lo, fmaf(hi, hi, s2)); SEND
    s1 = halfsum(s1); s2 = halfsum(s2);
    st8<SC1>(v2u{asu(s1), asu(s2)}, rsS, hh == 0 ? r * 8 : OOB, WS_HST + (btile * 14 + ct) * 256);
    if (b == 0 && threadIdx.x == 0) st4<SC1>(tag32, rsS, 0, WS_EPOCH);
    *(LAS v4u*)(L + L_HP + w * 2048 + vo) = v4u{hp[0], hp[1], hp[2], hp[3]};
    *(LAS v4u*)(L + L_HP + w * 2048 + 1024 + vo) = v4u{hp[4], hp[5], hp[6], hp[7]};
    SB();
  }
  STAMP(11);
  constexpr int WIN = DWIN;
  v4u xw[WIN];
  const int hb0 = WS_H1F + (2 * g) * 112 * 1024;
#define H1_OFF(m) (hb0 + ((m) >= 24 ? 112 * 1024 : 0) + D_KS(4 + (m) % 24) * 1024)
  {
    const int f1o = (lane < 14 ? ((8 * (lane >> 1) + 2 * w + (lane & 1)) * 7 + j) & 127 : 2 * fa + (lane & 3)) * 4;
    const float bv = bflo(b1[fa * 32 + lane]);
    SFOR(k, 0, ((72 - E3) / 2 > T5 - T4 ? (72 - E3) / 2 : T5 - T4))
      if constexpr (E3 + 2 * k < 72) { LD_RW(E3 + 2 * k); LD_RW(E3 + 2 * k + 1); }
      if constexpr (T4 + k < T5) TR(T4 + k);
      SB();
    SEND
    const unsigned f1 = ld4<SC1>(rsS, f1o, WS_FLAG1);
    unsigned cp[4];
    LAS float* c1x = (LAS float*)(L + L_C1) + w * 128;
    unsigned f3;
    {
      v4u hr[14]; v2u st[7];
#define H_LOADS() SFOR(i, 0, 14) hr[i] = ld16<SC1>(rsS, vo, WS_HF + ((2 * g) * 28 + 14 * w + i) * 1024); SEND \
        SFOR(i, 0, 7) st[i] = ld8<SC1>(rsS, lane * 8, WS_HST + ((2 * g + (w & 1)) * 14 + 2 * i) * 256); SEND
#define H_CHECK() unsigned m = 0; SFOR(i, 0, 14) m = vmax(m, hr[i]); \
          const int ks = 14 * (w & 1) + i - r1; \
          *(LAS v4u*)(L + L_H + ((w >> 1) * 28 + (ks < 0 ? ks + 28 : ks)) * 1024 + vo) = hr[i]; SEND \
        SFOR(i, 0, 7) m = umax3(m, st[i].x, st[i].y); SEND const bool again = anyl(m == POISON)
      H_LOADS()
      SB();
      SFOR(n, T5, 36) TR(n); SEND
      flag_wait(f1, rsS, f1o, WS_FLAG1, tag32);
      SFOR(q, 0, 4) cp[q] = ld4<SC1>(rsS, lane * 4, WS_C1P + q * (F * 4) + fa * 128); SEND
      SB();
      {
        H_CHECK();
        if (again) {
          for (int spin = 0; spin < SPIN_CAP; spin++) {
            MEMFENCE();
            H_LOADS()
            SB();
            H_CHECK();
            if (!again) break;
          }
        }
      }
      STAMP(12);
      float s1 = 0.f, s2 = 0.f;
      SFOR(i, 0, 7) s1 += asf(st[i].x); s2 += asf(st[i].y); SEND
      s1 = halfsum(s1); s2 = halfsum(s2);
      const float mean2 = s1 * (1.f / D), var2 = fmaxf(s2 * (1.f / D) - mean2 * mean2, 0.f);
      const float rs2 = __builtin_amdgcn_rsqf(var2 + 1e-5f);
      if (w < 2 && hh == 0) *(LAS f32x2*)(L + L_ST2 + w * 256 + r * 8) = f32x2{rs2, -mean2 * rs2};
    }
    BAR();
    STAMP(13);
    const f32x2 sv0 = *(LAS f32x2*)(L + L_ST2 + r * 8), sv1 = *(LAS f32x2*)(L + L_ST2 + 256 + r * 8);
    constexpr int LA = 4;
    v4u hf[LA];
    SFOR(i, 0, LA) hf[i] = lds16(L, L_H + i * 1024 + vo); SEND
    f32x16 aA0 = {}, aB0 = {}, aA1 = {}, aB1 = {};
    constexpr float C1 = -2.f * 0.7978845608028654f * LOG2E, C2 = C1 * 0.044715f;
    SFOR(i, 0, 28)
      aA0 = mfma32(wA[i], hf[i % LA], aA0);
      SB();
      LD_U(i);
      SB();
      aB0 = mfma32(wB[i], hf[i % LA], aB0);
      SB();
      hf[i % LA] = lds16(L, L_H + (i + LA) * 1024 + vo);
      if constexpr (i == 18) f3 = ld4<SC1>(rsS, (lane & 3) * 4, WS_FLAG3 + g * 128);
      if constexpr (i == 20) { c1x[lane] = (asf(cp[0]) + asf(cp[1])) + (asf(cp[2]) + asf(cp[3])); c1x[64 + lane] = bv; }
      SB();
    SEND
    flag_wait(f3, rsS, (lane & 3) * 4, WS_FLAG3 + g * 128, tag32);
    STAMP(14);
    f32x4 cc[2], bb[2];
    unsigned pkd[4];
    float ya[2], eb[2];
    v4u mine[2][4];
#define CONSTS(grp) do { cc[(grp) & 1] = *(LAS f32x4*)(c1x + 8 * (grp) + 4 * hh); bb[(grp) & 1] = *(LAS f32x4*)(c1x + 64 + 8 * (grp) + 4 * hh); } while (0)
#define GELU_SLOT(sl, AA, AB, SV, TILE) do { \
      constexpr int m7 = (sl) % 7, grp = (sl) / 7, pr = 2 * grp + (m7 >= 3 ? 1 : 0), stage = m7 >= 3 ? m7 - 3 : m7; \
      if constexpr (m7 == 3) CONSTS((grp + 1) & 7); \
      if constexpr (m7 < 6) { \
        SFOR(e, 0, 2) \
          constexpr int reg = (2 * pr + e) & 15; \
          if constexpr (stage == 0) { \
            ya[e] = fmaf((grp < 4 ? AA : AB)[reg], SV.x, fmaf(SV.y, cc[grp & 1][reg & 3], bb[grp & 1][reg & 3])); \
            eb[e] = fmaf(ya[e] * ya[e], C2, C1); \
          } else if constexpr (stage == 1) { \
            eb[e] = 1.f + __builtin_amdgcn_exp2f(ya[e] * eb[e]); \
          } else { \
            ya[e] = ya[e] * __builtin_amdgcn_rcpf(eb[e]); \
          } \
        SEND \
        if constexpr (stage == 2) pkd[pr & 3] = pk2(ya[0], ya[1]); \
        else pin4(ya[0], ya[1], eb[0], eb[1]); \
      } \
      if constexpr ((sl) % 14 == 13) { mine[TILE][(sl) / 14] = v4u{pkd[0], pkd[1], pkd[2], pkd[3]}; \
        st16<SC1>(mine[TILE][(sl) / 14], rsS, vo, hb0 + ((TILE) * 112 + 2 * fa + (sl) / 14) * 1024); } } while (0)
    CONSTS(0);
    SFOR(sl, 0, 56)
      constexpr int i = sl >> 1;
      if constexpr (sl & 1) {
        aB1 = mfma32(wB[i], hf[i % LA], aB1);
        SB();
        if constexpr (i + LA < 28) hf[i % LA] = lds16(L, L_H + (28 + i + LA) * 1024 + vo);
        LD_U(28 + i);
      } else {
        aA1 = mfma32(wA[i], hf[i % LA], aA1);
        SB();
      }
      if constexpr (sl == 40) f3 = ld4<SC1>(rsS, (lane & 3) * 4, WS_FLAG3 + g * 128 + 16);
      GELU_SLOT(sl, aA0, aB0, sv0, 0);
      SB();
    SEND
    STAMP(15);
    SFOR(n, 0, WIN) xw[n] = ld16<SC1>(rsS, vo, H1_OFF(n)); SEND
    flag_wait(f3, rsS, (lane & 3) * 4, WS_FLAG3 + g * 128 + 16, tag32);
    const rsrc_t rsB = mkrs(b2, D * 2), rsO = mkrs(out, S * D * 2);
    const int vd = opaque(vo);
    SB();
    SFOR(sl, 0, EP1) GELU_SLOT(sl, aA1, aB1, sv1, 1); SEND
    SB();
    STAMP(16);
    f32x16 a00 = {}, a10 = {}, a01 = {}, a11 = {}, own;
    v2u br[4];
    bool stale = false;
    SFOR(gq, 0, 28)
      constexpr int n = 2 * gq, i0 = n % 28, m = (n / 28) * 24 + i0 - 4;
      constexpr int nn = n + 2, i1 = nn % 28, m1 = (nn / 28) * 24 + i1 - 4;
      v4u y0, y1;
      if constexpr (i0 < 4) { y0 = mine[n / 28][i0]; y1 = mine[n / 28][i0 + 1]; }
      else {
        y0 = xw[m % WIN]; y1 = xw[(m + 1) % WIN];
        if (__builtin_expect(stale, 0)) {
          for (int spin = 0; spin < SPIN_CAP; spin++) {
            MEMFENCE();
            y0 = ld16<SC1>(rsS, vo, H1_OFF(m)); y1 = ld16<SC1>(rsS, vo, H1_OFF(m + 1));
            if (!anyl(vmax(vmax(0u, y0), y1) == POISON)) break;
          }
        }
      }
      SFOR(k, 0, 2)
        const v4u y = k ? y1 : y0;
        if constexpr (n < 28) a00 = mfma32(u0[n + k], y, a00); else a01 = mfma32(u0[n + k - 28], y, a01);
        SB();
        if constexpr (EP1 + 4 * gq + 2 * k < 56) GELU_SLOT(EP1 + 4 * gq + 2 * k, aA1, aB1, sv1, 1);
        if constexpr (k == 1 && i0 >= 4 && m + WIN < 48) xw[m % WIN] = ld16<SC1>(rsS, vo, H1_OFF(m + WIN));
        if constexpr (k == 0 && n >= 2) { constexpr int np = n - 1, ip = np % 28, mp = (np / 28) * 24 + ip - 4;
          if constexpr (ip >= 4 && mp + WIN < 48) xw[mp % WIN] = ld16<SC1>(rsS, vo, H1_OFF(mp + WIN)); }
        SB();
        if constexpr (n < 28) a10 = mfma32(u1[n + k], y, a10); else a11 = mfma32(u1[n + k - 28], y, a11);
        SB();
        if constexpr (EP1 + 4 * gq + 2 * k + 1 < 56) GELU_SLOT(EP1 + 4 * gq + 2 * k + 1, aA1, aB1, sv1, 1);
        if constexpr (k == 0 && nn < 56 && i1 >= 4) stale = anyl(vmax(vmax(0u, xw[m1 % WIN]), xw[(m1 + 1) % WIN]) == POISON);
        SB();
      SEND
      if constexpr (gq == 14) { BAR(); SFOR(q, 0, 4) br[q] = ld8<0>(rsB, hh * 8, ct * 64 + q * 16); SEND }
      if constexpr (gq == 16) PUT(L_H, vd, 0, a00);
      if constexpr (gq == 19) PUT(L_H, vd, 1, a10);
      if constexpr (gq == 0) STAMP(17);
      if constexpr (gq == 13) STAMP(18);
    SEND
    STAMP(19);
    PUT(L_H, vd, 2, a01); PUT(L_H, vd, 3, a11);
    const v4u hq0 = lds16(L, L_HP + w * 2048 + vd), hq1 = lds16(L, L_HP + w * 2048 + 1024 + vd);
    BAR();
    GET(L_H, vd)
    const unsigned hp[8] = {hq0.x, hq0.y, hq0.z, hq0.w, hq1.x, hq1.y, hq1.z, hq1.w};
    const int ln = vd >> 4;
    SFOR(q, 0, 4)
      v2u o;
      o.x = pk2(own[4 * q] + bflo(br[q].x) + bflo(hp[2 * q]), own[4 * q + 1] + bfhi(br[q].x) + bfhi(hp[2 * q]));
      o.y = pk2(own[4 * q + 2] + bflo(br[q].y) + bflo(hp[2 * q + 1]), own[4 * q + 3] + bfhi(br[q].y) + bfhi(hp[2 * q + 1]));
      *(LAS v2u*)(L + L_HP + w * 2048 + (ln & 31) * 64 + (ln >> 5) * 8 + ((q ^ ((ln >> 2) & 3)) * 16)) = o;
    SEND
    const int ow = L_HP + w * 2048 + (ln >> 2) * 64 + (((ln & 3) ^ (ln >> 4)) * 16), oo = (btile * 32 + (ln >> 2)) * 896 + ct * 64 + (ln & 3) * 16;
    st16<0>(lds16(L, ow), rsO, oo, 0);
    st16<0>(lds16(L, ow + 1024), rsO, oo, 16 * 896);
  }
}

__device__ __attribute__((used, noinline)) void text_end_marker() { asm volatile("fused_text_end:"); }

extern "C" void kernel_launch(void* const* d_in, const int* in_sizes, int n_in, void* d_out, int out_size, void* d_ws, size_t ws_size,
                              hipStream_t stream) {
  (void)hipFuncSetAttribute((const void*)fused, hipFuncAttributeMaxDynamicSharedMemorySize, L_BYTES);
  fused<<<256, 256, L_BYTES, stream>>>((const u16*)d_in[0], (const u16*)d_in[1], (const u16*)d_in[2], (const u16*)d_in[4], (const u16*)d_in[5],
                                       (const u16*)d_in[6], (const u16*)d_in[7], (u16*)d_out, (unsigned char*)d_ws);
}
```
